```python
import torch

def _softmax(x, axis=-1):
    return torch.softmax(x, dim=axis)
def _gelu(x):
    return torch.nn.functional.gelu(x, approximate="tanh")
def _silu(x):
    return torch.nn.functional.silu(x)
def _sigmoid(x):
    return torch.sigmoid(x)
def _layernorm(x, axis=-1, eps=1e-5):
    m = x.mean(dim=axis, keepdim=True)
    v = x.var(dim=axis, keepdim=True, unbiased=False)
    return (x - m) / torch.sqrt(v + eps)
def _scatter_add(tgt, idx, src, axis=0):
    return tgt.clone().index_add_(axis, idx, src)
def _segment_sum(src, seg, n):
    out = torch.zeros((n,) + src.shape[1:], dtype=src.dtype)
    return out.index_add_(0, seg, src)
def _pad(x, pw):
    flat = tuple(v for pair in reversed(pw) for v in pair)
    return torch.nn.functional.pad(x, flat)

kernel_name = 'concat_heads__gqa_block_cached__mha_block_cached__near_ridge'

def setup_inputs():

    _w = (lambda a: (lambda t: t.to(torch.bfloat16) if t.is_floating_point() else t)(torch.as_tensor(a)))
    def _A_setup():
        B, Sq, D = 16, 144, 640
        Hq, Hkv, Sk, Dh = 32, 32, 512, 192
        assert Hq % Hkv == 0, (Hq, Hkv)
        return {
            "x": _w((lambda *s: torch.randn(*s, dtype=torch.bfloat16) * 0.8)(B, Sq, D)),
            "k_cache": _w((lambda *s: torch.randn(*s, dtype=torch.bfloat16) * 0.8)(B, Hkv, Sk, Dh)),
            "v_cache": _w((lambda *s: torch.randn(*s, dtype=torch.bfloat16) * 0.05)(B, Hkv, Sk, Dh)),
            "wq": _w((lambda *s: torch.randn(*s, dtype=torch.bfloat16) * 0.8)(D, Hq * Dh) * (D ** -0.5)),
            "wo": _w((lambda *s: torch.randn(*s, dtype=torch.bfloat16) * 0.05)(Hq * Dh, D) * ((Hq * Dh) ** -0.5)),
        }
    _A_in = _A_setup()

    def _B_setup():
        B, Sq, D = 16, 144, 640
        H, Sk, Dh = 32, 512, 192
        return {
            "x": _w((lambda *s: torch.randn(*s, dtype=torch.bfloat16) * 0.8)(B, Sq, D)),
            "k_cache": _w((lambda *s: torch.randn(*s, dtype=torch.bfloat16) * 0.8)(B, H, Sk, Dh)),
            "v_cache": _w((lambda *s: torch.randn(*s, dtype=torch.bfloat16) * 0.05)(B, H, Sk, Dh)),
            "wq": _w((lambda *s: torch.randn(*s, dtype=torch.bfloat16) * 0.8)(D, H * Dh) * (D ** -0.5)),
            "wo": _w((lambda *s: torch.randn(*s, dtype=torch.bfloat16) * 0.05)(H * Dh, D) * ((H * Dh) ** -0.5)),
        }
    _B_in = _B_setup()

    return {
        "A_x": _A_in["x"],
        "A_k_cache": _A_in["k_cache"],
        "A_v_cache": _A_in["v_cache"],
        "A_wq": _A_in["wq"],
        "A_wo": _A_in["wo"],
        "B_x": _B_in["x"],
        "B_k_cache": _B_in["k_cache"],
        "B_v_cache": _B_in["v_cache"],
        "B_wq": _B_in["wq"],
        "B_wo": _B_in["wo"],
    }

def reference(A_x, A_k_cache, A_v_cache, A_wq, A_wo, B_x, B_k_cache, B_v_cache, B_wq, B_wo):
    def _A_ref(x, k_cache, v_cache, wq, wo):
        B, Sq, D = x.shape
        Hq, Dh = 32, 192
        Hkv = k_cache.shape[1]
        rep = Hq // Hkv
        q = (lambda x, *d: x.permute(*d))((x @ wq).reshape(B, Sq, Hq, Dh), 0, 2, 1, 3)
        k = (lambda x, n, axis: torch.repeat_interleave(x, n, dim=axis))(k_cache, rep, axis=1)
        v = (lambda x, n, axis: torch.repeat_interleave(x, n, dim=axis))(v_cache, rep, axis=1)
        scores = torch.einsum("bhqd,bhkd->bhqk", q, k) * (Dh ** -0.5)
        p = _softmax(scores, axis=-1)
        o = torch.einsum("bhqk,bhkd->bhqd", p, v)
        return (lambda x, *d: x.permute(*d))(o, 0, 2, 1, 3).reshape(B, Sq, Hq * Dh) @ wo

    def _B_ref(x, k_cache, v_cache, wq, wo):
        B, Sq, D = x.shape
        H, Dh = 32, 192
        q = (lambda x, *d: x.permute(*d))((x @ wq).reshape(B, Sq, H, Dh), 0, 2, 1, 3)
        scores = torch.einsum("bhqd,bhkd->bhqk", q, k_cache) * (Dh ** -0.5)
        p = _softmax(scores, axis=-1)
        o = torch.einsum("bhqk,bhkd->bhqd", p, v_cache)
        return (lambda x, *d: x.permute(*d))(o, 0, 2, 1, 3).reshape(B, Sq, H * Dh) @ wo

    a = _A_ref(A_x, A_k_cache, A_v_cache, A_wq, A_wo)
    b = _B_ref(B_x, B_k_cache, B_v_cache, B_wq, B_wo)
    return torch.concatenate([a, b], axis=-1)
```

```cpp
#include <hip/hip_runtime.h>
#include <hip/hip_bf16.h>
typedef __hip_bfloat16 bf16;
__global__ void qproj(const bf16* x, const bf16* wq, bf16* q) {
  int n = blockIdx.x * 256 + threadIdx.x; int m = blockIdx.y;
  float acc = 0;
  for (int c = 0; c < 640; ++c) acc += __bfloat162float(x[m * 640 + c]) * __bfloat162float(wq[c * 6144 + n]);
  q[(size_t)m * 6144 + n] = __float2bfloat16(acc);
}
__global__ void attn(const bf16* q, const bf16* k, const bf16* v, bf16* o) {
  int row = blockIdx.x; int h = blockIdx.y; int b = row / 144;
  __shared__ float s[512]; __shared__ float qs[192]; __shared__ float red[256];
  int t = threadIdx.x;
  if (t < 192) qs[t] = __bfloat162float(q[(size_t)row * 6144 + h * 192 + t]);
  __syncthreads();
  const bf16* kb = k + ((size_t)(b * 32 + h) * 512) * 192; const bf16* vb = v + ((size_t)(b * 32 + h) * 512) * 192;
  float mx = -1e30f;
  for (int j = t; j < 512; j += 256) { float a = 0; for (int d = 0; d < 192; ++d) a += qs[d] * __bfloat162float(kb[j * 192 + d]); a *= 0.07216878364870322f; s[j] = a; mx = fmaxf(mx, a); }
  red[t] = mx; __syncthreads();
  for (int o2 = 128; o2 > 0; o2 >>= 1) { if (t < o2) red[t] = fmaxf(red[t], red[t + o2]); __syncthreads(); }
  mx = red[0]; __syncthreads();
  float sum = 0; for (int j = t; j < 512; j += 256) { float e = __expf(s[j] - mx); s[j] = e; sum += e; }
  red[t] = sum; __syncthreads();
  for (int o2 = 128; o2 > 0; o2 >>= 1) { if (t < o2) red[t] += red[t + o2]; __syncthreads(); }
  sum = red[0];
  if (t < 192) { float a = 0; for (int j = 0; j < 512; ++j) a += s[j] * __bfloat162float(vb[j * 192 + t]); o[(size_t)row * 6144 + h * 192 + t] = __float2bfloat16(a / sum); }
}
__global__ void oproj(const bf16* o, const bf16* wo, bf16* out, int coff) {
  int n = blockIdx.x * 128 + threadIdx.x; int m = blockIdx.y;
  float acc = 0;
  for (int c = 0; c < 6144; ++c) acc += __bfloat162float(o[(size_t)m * 6144 + c]) * __bfloat162float(wo[c * 640 + n]);
  out[(size_t)m * 1280 + coff + n] = __float2bfloat16(acc);
}
extern "C" void kernel_launch(void* const* d_in, const int* in_sizes, int n_in, void* d_out, int out_size, void* d_ws, size_t ws_size, hipStream_t stream) {
  bf16* q = (bf16*)d_ws; bf16* o = q + (size_t)2304 * 6144;
  for (int br = 0; br < 2; ++br) {
    const bf16* x = (const bf16*)d_in[br * 5 + 0]; const bf16* k = (const bf16*)d_in[br * 5 + 1]; const bf16* v = (const bf16*)d_in[br * 5 + 2];
    const bf16* wq = (const bf16*)d_in[br * 5 + 3]; const bf16* wo = (const bf16*)d_in[br * 5 + 4];
    qproj<<<dim3(24, 2304), 256, 0, stream>>>(x, wq, q);
    attn<<<dim3(2304, 32), 256, 0, stream>>>(q, k, v, o);
    oproj<<<dim3(5, 2304), 128, 0, stream>>>(o, wo, (bf16*)d_out, br * 640);
  }
}
```

```cpp
#include <hip/hip_runtime.h>
#include <hip/hip_bf16.h>
#include <stdint.h>
#include <utility>
#include <type_traits>

typedef __hip_bfloat16 bf16;
typedef short bf16x8 __attribute__((ext_vector_type(8)));
typedef short s16x4 __attribute__((ext_vector_type(4)));
typedef float f32x16 __attribute__((ext_vector_type(16)));
typedef float f32x4 __attribute__((ext_vector_type(4)));
typedef float f32x2 __attribute__((ext_vector_type(2)));
typedef unsigned u32x4 __attribute__((ext_vector_type(4)));
typedef __bf16 bf2_t __attribute__((ext_vector_type(2)));
typedef __attribute__((address_space(3))) char lds_char;
typedef __attribute__((address_space(3))) s16x4 lds_s16x4;
typedef __attribute__((address_space(3))) bf16x8 lds_bf16x8;

#define DI __device__ __forceinline__
#define MFMA32(a, b, c) __builtin_amdgcn_mfma_f32_32x32x16_bf16((a), (b), (c), 0, 0, 0)
#define MFMA16(a, b, c) __builtin_amdgcn_mfma_f32_16x16x32_bf16((a), (b), (c), 0, 0, 0)
#define SB() __builtin_amdgcn_sched_barrier(0)
#define PIN(x) asm volatile("" : "+v"(x))

constexpr int NB = 16, SQ = 144, DM = 640, NH = 32, SK = 512, DH = 192, HD = NH * DH;
constexpr int CHB = 12288;
constexpr int XB = 9216;
constexpr int LDS_K = 0, LDS_V = 3 * CHB, LDS_WX = 6 * CHB, WXS = CHB + XB;
constexpr int LDS_Q = LDS_WX + WXS;
constexpr int LDS_SCR = LDS_WX + 4 * WXS;
constexpr int LDS_TOTAL = LDS_SCR + 256;
constexpr float QSCALE = 0.07216878364870322f * 1.4426950408889634f;
#define KVPOS 2
#define WXPOS 0
#define NBX 8
#define TOUCH_AHEAD 2
constexpr float THR = 8.0f;
constexpr size_t O_PER_B = (size_t)SQ * HD * 2;

DI unsigned cvtpk(float lo, float hi) {
  f32x2 v = {lo, hi};
  bf2_t b = __builtin_convertvector(v, bf2_t);
  return __builtin_bit_cast(unsigned, b);
}
DI bf16x8 pack8(float a0, float a1, float a2, float a3, float a4, float a5, float a6, float a7) {
  u32x4 w = {cvtpk(a0, a1), cvtpk(a2, a3), cvtpk(a4, a5), cvtpk(a6, a7)};
  return __builtin_bit_cast(bf16x8, w);
}
#define OSTORE(v, p) __builtin_nontemporal_store((v), (bf16x8*)(p))
DI s16x4 trread(const lds_char* p) { return __builtin_amdgcn_ds_read_tr16_b64_v4i16((lds_s16x4*)p); }
DI bf16x8 tr2(const lds_char* p0, const lds_char* p1) {
  s16x4 lo = trread(p0), hi = trread(p1);
  return __builtin_shufflevector(lo, hi, 0, 1, 2, 3, 4, 5, 6, 7);
}
DI bf16x8 ldsr(const lds_char* p) { return *(const lds_bf16x8*)p; }

template <int NT, int IMM = 0>
DI void dma16(const char* sbase, unsigned voff, unsigned ldsdst) {
  unsigned keep;
  if constexpr (NT)
    asm volatile("s_mov_b32 %0, m0\n\ts_add_u32 m0, %3, %4\n\ts_nop 0\n\tglobal_load_lds_dwordx4 %1, %2 nt\n\ts_mov_b32 m0, %0"
                 : "=&s"(keep) : "v"(voff), "s"(sbase), "s"(ldsdst), "i"(IMM) : "memory", "scc");
  else
    asm volatile("s_mov_b32 %0, m0\n\ts_add_u32 m0, %3, %4\n\ts_nop 0\n\tglobal_load_lds_dwordx4 %1, %2\n\ts_mov_b32 m0, %0"
                 : "=&s"(keep) : "v"(voff), "s"(sbase), "s"(ldsdst), "i"(IMM) : "memory", "scc");
}
DI void touch4(const char* sbase, unsigned voff, unsigned ldsdst) {
  unsigned keep;
  asm volatile("s_mov_b32 %0, m0\n\ts_mov_b32 m0, %3\n\ts_nop 0\n\tglobal_load_lds_dword %1, %2\n\ts_mov_b32 m0, %0"
               : "=&s"(keep) : "v"(voff), "s"(sbase), "s"(ldsdst) : "memory");
}
#define WAIT_BAR(N) do { SB(); asm volatile("s_waitcnt vmcnt(" #N ") lgkmcnt(0)\n\ts_barrier" ::: "memory"); SB(); } while (0)

DI unsigned swv(unsigned row) { return (((row >> 1) & 1) << 2) | (((row >> 2) & 1) << 1); }
DI float max3(float a, float b, float c) { return __builtin_fmaxf(__builtin_fmaxf(a, b), c); }
DI float xhalf_max(float m) {
  auto rr = __builtin_amdgcn_permlane32_swap(__float_as_uint(m), __float_as_uint(m), false, false);
  return __builtin_fmaxf(__uint_as_float(rr[0]), __uint_as_float(rr[1]));
}
DI float xhalf_sum(float m) {
  auto rr = __builtin_amdgcn_permlane32_swap(__float_as_uint(m), __float_as_uint(m), false, false);
  return __uint_as_float(rr[0]) + __uint_as_float(rr[1]);
}
DI float x16_max(float m) {
  auto rr = __builtin_amdgcn_permlane16_swap(__float_as_uint(m), __float_as_uint(m), false, false);
  return __builtin_fmaxf(__uint_as_float(rr[0]), __uint_as_float(rr[1]));
}
DI float x16_sum(float m) {
  auto rr = __builtin_amdgcn_permlane16_swap(__float_as_uint(m), __float_as_uint(m), false, false);
  return __uint_as_float(rr[0]) + __uint_as_float(rr[1]);
}
template <int N, class F, int... Is> DI void static_for_impl(F&& f, std::integer_sequence<int, Is...>) { (f(std::integral_constant<int, Is>{}), ...); }
template <int N, class F> DI void static_for(F&& f) { static_for_impl<N>(f, std::make_integer_sequence<int, N>{}); }
DI int inc3(int x) { return x == 2 * CHB ? 0 : x + CHB; }

struct Ctx {
  const char* kg;
  const char* vg;
  const char* wq;
  const char* xg;
  char* og;
  int h0;
  int b, xs;
};

DI void main_wave(const Ctx& c, lds_char* lds, int w, int lane) {
  const unsigned lds0 = (unsigned)(uintptr_t)lds;
  const int r32 = lane & 31, h2 = lane >> 5;
  unsigned koff[3], voff[3];
#pragma unroll
  for (int ii = 0; ii < 3; ++ii) {
    unsigned p = 64u * (3 * w + ii) + lane, row = (p >> 3) / 3;
    koff[ii] = 16u * (p ^ ((row >> 1) & 7));
    voff[ii] = 16u * (p ^ swv(row));
  }
  const unsigned dk = lds0 + LDS_K + 3072u * w, dv = lds0 + LDS_V + 3072u * w;
  unsigned kb[4];
#pragma unroll
  for (int s = 0; s < 4; ++s) kb[s] = LDS_K + 384u * r32 + 16u * ((2u * s + h2) ^ ((r32 >> 1) & 7));
  unsigned vb[2];
  {
    const unsigned blk = (lane >> 4) & 1, q4 = (lane & 15) >> 2, p = lane & 3;
#pragma unroll
    for (int t = 0; t < 2; ++t)
      vb[t] = LDS_V + 384u * (4u * h2 + q4) + 16u * (4u * (t ^ (q4 >> 1)) + 2u * (blk ^ h2) + (p >> 1)) + 8u * (p & 1);
  }

  bf16x8 qf[12];
  f32x16 o[6];
#pragma unroll
  for (int t = 0; t < 6; ++t) o[t] = f32x16{};
  float m = 0.f, mc = 0.f, l = 0.f, alpha = 1.f;
  bool resc = false;
  bf16x8 pf0 = {}, pf1 = {};
  bf16x8 f0 = {}, f1 = {}, f2 = {}, f3 = {}, f4 = {}, f5 = {};
  f32x16 sA = {}, sB = {};
  int ks_rd = 0;
  int vs_rd = 0;
  const char* kptr = c.kg;
  const char* vptr = c.vg;
  int ks_wr = 0, vs_wr = 0;

#define KRD(ks) ldsr(lds + ks_rd + kb[(ks) & 3] + 128 * ((ks) >> 2))
#define VRD(j) tr2(lds + vs_rd + vb[((j) % 6) & 1] + 128 * (((j) % 6) >> 1) + 6144 * ((j) / 6), lds + vs_rd + vb[((j) % 6) & 1] + 128 * (((j) % 6) >> 1) + 6144 * ((j) / 6) + 3072)
#define DMAK(ii) dma16<1, 1024 * (ii)>(kptr, koff[ii], dk + ks_wr)
#define DMAV(ii) dma16<1, 1024 * (ii)>(vptr, voff[ii], dv + vs_wr)
#define ADVK() do { kptr += CHB; ks_wr = inc3(ks_wr); } while (0)
#define ADVV() do { vptr += CHB; vs_wr = inc3(vs_wr); } while (0)
#define EXP2M(x) __builtin_amdgcn_exp2f(__builtin_fmaf((x), QSCALE, mc))

  auto rescale = [&]() {
    asm volatile("" ::: "memory");
    if (__builtin_expect(resc, 0)) {
#pragma unroll
      for (int tt = 0; tt < 6; ++tt)
#pragma unroll
        for (int r = 0; r < 16; ++r) o[tt][r] *= alpha;
      resc = false;
    }
  };
  auto decide = [&](float mx) {
    asm volatile("" ::: "memory");
    if (__builtin_expect(__any(mx - m > THR / QSCALE), 0)) {
      float mn = __builtin_fmaxf(m, mx);
      alpha = __builtin_amdgcn_exp2f((m - mn) * QSCALE);
      m = mn; mc = -mn * QSCALE; l *= alpha; resc = true;
    }
  };
  auto step = [&](f32x16& sN, f32x16& sP, bool gk, bool gv) {
    o[0] = MFMA32(f0, pf1, o[0]); f0 = KRD(0); sP[6] = EXP2M(sP[6]); sP[7] = EXP2M(sP[7]); PIN(sP); SB();
    o[1] = MFMA32(f1, pf1, o[1]); f1 = KRD(1); sP[8] = EXP2M(sP[8]); sP[9] = EXP2M(sP[9]); PIN(sP); SB();
    o[2] = MFMA32(f2, pf1, o[2]); f2 = KRD(2); sP[10] = EXP2M(sP[10]); sP[11] = EXP2M(sP[11]); PIN(sP); SB();
    o[3] = MFMA32(f3, pf1, o[3]); f3 = KRD(3); sP[12] = EXP2M(sP[12]); sP[13] = EXP2M(sP[13]); PIN(sP); SB();
    o[4] = MFMA32(f4, pf1, o[4]); f4 = KRD(4); sP[14] = EXP2M(sP[14]); sP[15] = EXP2M(sP[15]); PIN(sP); SB();
    o[5] = MFMA32(f5, pf1, o[5]); f5 = KRD(5); pf0 = pack8(sP[0], sP[1], sP[2], sP[3], sP[4], sP[5], sP[6], sP[7]); PIN(pf0); SB();
    float sum;
    sN = MFMA32(f0, qf[0], (f32x16{})); f0 = KRD(6); sum = sP[0] + sP[1]; sum += sP[2]; sum += sP[3]; PIN(sum); SB();
    sN = MFMA32(f1, qf[1], sN); f1 = KRD(7); pf1 = pack8(sP[8], sP[9], sP[10], sP[11], sP[12], sP[13], sP[14], sP[15]); PIN(pf1); SB();
    sN = MFMA32(f2, qf[2], sN); f2 = KRD(8); sum += sP[4]; sum += sP[5]; sum += sP[6]; sum += sP[7]; PIN(sum); SB();
    sN = MFMA32(f3, qf[3], sN); f3 = KRD(9); sum += sP[8]; sum += sP[9]; sum += sP[10]; sum += sP[11]; PIN(sum); SB();
    sN = MFMA32(f4, qf[4], sN); f4 = KRD(10); SB();
    sN = MFMA32(f5, qf[5], sN); f5 = KRD(11); sum += sP[12]; sum += sP[13]; sum += sP[14]; sum += sP[15]; l += sum; PIN(l); SB();
    rescale();
    SB();
    sN = MFMA32(f0, qf[6], sN); f0 = VRD(0); SB();
    sN = MFMA32(f1, qf[7], sN); f1 = VRD(1); SB();
    sN = MFMA32(f2, qf[8], sN); f2 = VRD(2); SB();
    sN = MFMA32(f3, qf[9], sN); f3 = VRD(3); SB();
    sN = MFMA32(f4, qf[10], sN); f4 = VRD(4); SB();
    sN = MFMA32(f5, qf[11], sN); f5 = VRD(5); SB();
    o[0] = MFMA32(f0, pf0, o[0]); f0 = VRD(6); SB();
    o[1] = MFMA32(f1, pf0, o[1]); f1 = VRD(7); SB();
    float mx = max3(sN[0], sN[1], sN[2]), mx2 = max3(sN[3], sN[4], sN[5]);
    mx = max3(mx, sN[6], sN[7]); mx2 = max3(mx2, sN[8], sN[9]);
    o[2] = MFMA32(f2, pf0, o[2]); f2 = VRD(8);
    mx = max3(mx, sN[10], sN[11]); mx2 = max3(mx2, sN[12], sN[13]); mx = max3(mx, sN[14], sN[15]); mx = __builtin_fmaxf(mx, mx2);
    mx = xhalf_max(mx);
    SB();
    decide(mx);
    SB();
    o[3] = MFMA32(f3, pf0, o[3]); f3 = VRD(9); sN[0] = EXP2M(sN[0]); sN[1] = EXP2M(sN[1]); PIN(sN); SB();
    o[4] = MFMA32(f4, pf0, o[4]); f4 = VRD(10); sN[2] = EXP2M(sN[2]); sN[3] = EXP2M(sN[3]); PIN(sN); SB();
    o[5] = MFMA32(f5, pf0, o[5]); f5 = VRD(11); sN[4] = EXP2M(sN[4]); sN[5] = EXP2M(sN[5]); PIN(sN); SB();
    if (gk) { DMAK(0); DMAK(1); DMAK(2); }
    if (gv) { DMAV(0); DMAV(1); DMAV(2); }
    if (gk) ADVK();
    if (gv) ADVV();
    ks_rd = inc3(ks_rd); vs_rd = inc3(vs_rd);
  };
  auto readq = [&]() {
    unsigned qb = LDS_Q + w * 12288 + lane * 16;
    PIN(qb);
#pragma unroll
    for (int ks = 0; ks < 12; ++ks) qf[ks] = ldsr(lds + qb + ks * 1024);
  };
  auto newhead = [&](auto epi_, float inv, char* op) {
    constexpr bool EPI = decltype(epi_)::value;
    f0 = KRD(0); f1 = KRD(1); f2 = KRD(2); f3 = KRD(3); f4 = KRD(4); f5 = KRD(5);
    SB();
    static_for<12>([&](auto k_) {
      constexpr int k = decltype(k_)::value;
      bf16x8& f = (k % 6 == 0) ? f0 : (k % 6 == 1) ? f1 : (k % 6 == 2) ? f2 : (k % 6 == 3) ? f3 : (k % 6 == 4) ? f4 : f5;
      if constexpr (k == 0) sA = MFMA32(f, qf[0], (f32x16{})); else sA = MFMA32(f, qf[k], sA);
      if constexpr (k < 6) f = KRD(k + 6); else f = bf16x8{};
      if constexpr (EPI) {
        constexpr int tt = k >> 1, sp = k & 1;
        bf16x8 v = pack8(o[tt][8 * sp] * inv, o[tt][8 * sp + 1] * inv, o[tt][8 * sp + 2] * inv, o[tt][8 * sp + 3] * inv,
                         o[tt][8 * sp + 4] * inv, o[tt][8 * sp + 5] * inv, o[tt][8 * sp + 6] * inv, o[tt][8 * sp + 7] * inv);
        OSTORE(v, (op + k * 1024) + (unsigned)(lane * 16));
        if constexpr (sp == 1) { o[tt] = f32x16{}; PIN(o[tt]); }
      }
      SB();
    });
    float mx = max3(sA[0], sA[1], sA[2]);
    mx = max3(mx, sA[3], sA[4]); mx = max3(mx, sA[5], sA[6]); mx = max3(mx, sA[7], sA[8]); mx = max3(mx, sA[9], sA[10]);
    mx = max3(mx, sA[11], sA[12]); mx = max3(mx, sA[13], sA[14]); mx = __builtin_fmaxf(mx, sA[15]);
    m = xhalf_max(mx); mc = -m * QSCALE; l = 0.f; resc = false;
#pragma unroll
    for (int r = 0; r < 6; ++r) sA[r] = EXP2M(sA[r]);
    PIN(sA);
    ks_rd = inc3(ks_rd);
  };

  {
    unsigned wo[3], xo[3];
#pragma unroll
    for (int ii = 0; ii < 3; ++ii) {
      unsigned pp = 64u * (3 * w + ii) + lane, row = (pp >> 3) / 3, c16 = (pp ^ swv(row)) - 24u * row;
      wo[ii] = row * (HD * 2) + 16u * c16;
      unsigned r = 16u * (w + 4 * ii) + (lane >> 2);
      xo[ii] = r * (DM * 2) + 16u * ((lane & 3) ^ ((r >> 2) & 3));
    }
    auto pair_issue = [&](int p) {
      const int pr = (p + 1) % 3;
      const unsigned base = lds0 + (pr == 2 ? 0 : LDS_WX + 2 * pr * WXS);
      const unsigned bw = base + 3072u * w, bx = base + CHB + 1024u * w;
      const char* sw = c.wq + (size_t)p * (64 * HD * 2);
      const char* sx = c.xg + p * 128;
      dma16<0, 0>(sw, wo[0], bw); dma16<0, 1024>(sw, wo[1], bw); dma16<0, 2048>(sw, wo[2], bw);
      dma16<0, 0>(sx, xo[0], bx); dma16<0, 4096>(sx, xo[1], bx); if (w == 0) dma16<0, 8192>(sx, xo[2], bx);
      sw += 32 * HD * 2; sx += 64;
      dma16<0, WXS>(sw, wo[0], bw); dma16<0, WXS + 1024>(sw, wo[1], bw); dma16<0, WXS + 2048>(sw, wo[2], bw);
      dma16<0, WXS>(sx, xo[0], bx); dma16<0, WXS + 4096>(sx, xo[1], bx); if (w == 0) dma16<0, WXS + 8192>(sx, xo[2], bx);
    };
    pair_issue(0); pair_issue(1);
    WAIT_BAR(10);
    for (int p = 0; p < 8; ++p) {
      pair_issue(p + 2);
      if (p >= 6) {
        const char* vp = c.vg + (size_t)(p - 5) * CHB; const unsigned d = dv + (p - 5) * CHB;
        dma16<1, 0>(vp, voff[0], d); dma16<1, 1024>(vp, voff[1], d); dma16<1, 2048>(vp, voff[2], d);
        WAIT_BAR(13);
      } else { WAIT_BAR(10); }
    }
    DMAK(0); DMAK(1); DMAK(2); ADVK(); DMAV(0); DMAV(1); DMAV(2);
    DMAK(0); DMAK(1); DMAK(2); ADVK();
    vptr += 3 * CHB;
    WAIT_BAR(9);
    DMAK(0); DMAK(1); DMAK(2); ADVK();
    WAIT_BAR(9);
    WAIT_BAR(63);
    readq();
    WAIT_BAR(63);
    newhead(std::false_type{}, 0.f, nullptr);
    WAIT_BAR(3);
  }

  for (int e = 0; e < 4; ++e) {
    const bool last = (e == 3);
    step(sB, sA, true, e > 0);
    if (e == 0) { WAIT_BAR(3); } else { WAIT_BAR(18); }
    for (int i = 1; i < 13; i += 2) {
      step(sA, sB, true, true); WAIT_BAR(6);
      step(sB, sA, true, true); WAIT_BAR(6);
    }
    step(sA, sB, !last, true);  if (last) { WAIT_BAR(3); } else { WAIT_BAR(6); }
    step(sB, sA, !last, !last); if (last) { WAIT_BAR(0); } else { WAIT_BAR(6); }
    {
      if (!last) { DMAK(0); DMAK(1); DMAK(2); ADVK(); DMAV(0); DMAV(1); DMAV(2); ADVV(); }
      SB();
      o[0] = MFMA32(f0, pf1, o[0]); f0 = VRD(0); sB[6] = EXP2M(sB[6]); sB[7] = EXP2M(sB[7]); PIN(sB); SB();
      o[1] = MFMA32(f1, pf1, o[1]); f1 = VRD(1); sB[8] = EXP2M(sB[8]); sB[9] = EXP2M(sB[9]); PIN(sB); SB();
      o[2] = MFMA32(f2, pf1, o[2]); f2 = VRD(2); pf0 = pack8(sB[0], sB[1], sB[2], sB[3], sB[4], sB[5], sB[6], sB[7]); PIN(pf0); SB();
      o[3] = MFMA32(f3, pf1, o[3]); f3 = VRD(3); sB[10] = EXP2M(sB[10]); sB[11] = EXP2M(sB[11]); PIN(sB); SB();
      o[4] = MFMA32(f4, pf1, o[4]); f4 = VRD(4); sB[12] = EXP2M(sB[12]); sB[13] = EXP2M(sB[13]); PIN(sB); SB();
      o[5] = MFMA32(f5, pf1, o[5]); f5 = VRD(5); sB[14] = EXP2M(sB[14]); sB[15] = EXP2M(sB[15]); PIN(sB); SB();
      rescale();
      SB();
      float sum;
      o[0] = MFMA32(f0, pf0, o[0]); f0 = VRD(6); pf1 = pack8(sB[8], sB[9], sB[10], sB[11], sB[12], sB[13], sB[14], sB[15]); PIN(pf1); SB();
      o[1] = MFMA32(f1, pf0, o[1]); f1 = VRD(7); sum = sB[0] + sB[1]; sum += sB[2]; sum += sB[3]; PIN(sum); SB();
      o[2] = MFMA32(f2, pf0, o[2]); f2 = VRD(8); sum += sB[4]; sum += sB[5]; sum += sB[6]; sum += sB[7]; PIN(sum); SB();
      o[3] = MFMA32(f3, pf0, o[3]); f3 = VRD(9); sum += sB[8]; sum += sB[9]; sum += sB[10]; sum += sB[11]; PIN(sum); SB();
      o[4] = MFMA32(f4, pf0, o[4]); f4 = VRD(10); sum += sB[12]; sum += sB[13]; sum += sB[14]; sum += sB[15]; l += sum; PIN(l); SB();
      o[5] = MFMA32(f5, pf0, o[5]); f5 = VRD(11); SB();
      float inv = 1.0f / xhalf_sum(l);
      o[0] = MFMA32(f0, pf1, o[0]); o[1] = MFMA32(f1, pf1, o[1]); o[2] = MFMA32(f2, pf1, o[2]);
      o[3] = MFMA32(f3, pf1, o[3]); o[4] = MFMA32(f4, pf1, o[4]); o[5] = MFMA32(f5, pf1, o[5]);
      PIN(inv);
      vs_rd = inc3(vs_rd);
      char* op = c.og + (size_t)(w * 384 + (c.h0 + e) * 12) * 1024;
      if (!last) {
        WAIT_BAR(63);
        readq();
        WAIT_BAR(63);
      }
      newhead(std::true_type{}, inv, op);
      if (last) break;
      WAIT_BAR(18);
    }
  }
#undef KRD
#undef VRD
#undef DMAK
#undef DMAV
#undef ADVK
#undef ADVV
#undef EXP2M
}

struct WxDma {
  unsigned woff[4], xoff[3];
  unsigned dw, dx;
  DI void init(unsigned lds0, int j, int lane) {
#pragma unroll
    for (int ii = 0; ii < 4; ++ii) {
      unsigned p = 64u * (4 * (j - 1) + ii) + lane, row = (p >> 3) / 3;
      unsigned cc = (p ^ swv(row)) - 24u * row;
      woff[ii] = row * (HD * 2) + 16u * cc;
    }
#pragma unroll
    for (int ii = 0; ii < 3; ++ii) {
      unsigned xi = 3u * (j - 1) + ii;
      unsigned r = 16u * xi + (lane >> 2);
      xoff[ii] = r * (DM * 2) + 16u * ((lane & 3) ^ ((r >> 2) & 3));
    }
    dw = lds0 + LDS_WX + 4096u * (j - 1); dx = lds0 + LDS_WX + CHB + 3072u * (j - 1);
  }
  template <int II> DI void w(const Ctx& c, int e, int ch, int slot) const {
    dma16<0, 1024 * II>(c.wq + (size_t)ch * (32 * HD * 2) + e * (DH * 2), woff[II], dw + slot * WXS);
  }
  template <int II> DI void x(const Ctx& c, int ch, int slot) const { dma16<0, 1024 * II>(c.xg + ch * 64, xoff[II], dx + slot * WXS); }
};

DI void proj_wave(const Ctx& c, lds_char* lds, int j, int lane) {
  const unsigned lds0 = (unsigned)(uintptr_t)lds;
  const int r32 = lane & 31, h2 = lane >> 5;
  WxDma dma; dma.init(lds0, j, lane);
  unsigned wb[2][2];
  {
    const unsigned blk = (lane >> 4) & 1, q4 = (lane & 15) >> 2, p = lane & 3;
#pragma unroll
    for (int tt = 0; tt < 2; ++tt)
#pragma unroll
      for (int a = 0; a < 2; ++a)
        wb[tt][a] = LDS_WX + 384u * (8u * h2 + 4u * a + q4) + 128u * (j - 1) +
                    16u * (4u * (tt ^ (q4 >> 1)) + 2u * (blk ^ a) + (p & 1)) + 8u * (p >> 1);
  }
  unsigned xb[2];
#pragma unroll
  for (int sp = 0; sp < 2; ++sp) xb[sp] = LDS_WX + CHB + 64u * r32 + 16u * ((2u * sp + h2) ^ ((r32 >> 2) & 3));
  f32x16 acc[4][2];
  bf16x8 aA[2], bA[4], aB[2], bB[4];
#define KSTEP(ar, br, slot, sp, am, bm, D0, D1, D2, D3, D4, D5, D6) do { \
    const lds_char* bs_ = lds + (slot) * WXS; \
    acc[0][0] = MFMA32(am[0], bm[0], acc[0][0]); ar[0] = tr2(bs_ + wb[0][0] + 6144 * (sp), bs_ + wb[0][1] + 6144 * (sp)); SB(); \
    acc[0][1] = MFMA32(am[1], bm[0], acc[0][1]); br[0] = ldsr(bs_ + xb[sp]); D0; SB(); \
    acc[1][0] = MFMA32(am[0], bm[1], acc[1][0]); ar[1] = tr2(bs_ + wb[1][0] + 6144 * (sp), bs_ + wb[1][1] + 6144 * (sp)); D1; SB(); \
    acc[1][1] = MFMA32(am[1], bm[1], acc[1][1]); br[1] = ldsr(bs_ + xb[sp] + 2048); D2; SB(); \
    acc[2][0] = MFMA32(am[0], bm[2], acc[2][0]); br[2] = ldsr(bs_ + xb[sp] + 4096); D3; SB(); \
    acc[2][1] = MFMA32(am[1], bm[2], acc[2][1]); br[3] = ldsr(bs_ + xb[sp] + 6144); D4; SB(); \
    acc[3][0] = MFMA32(am[0], bm[3], acc[3][0]); D5; SB(); \
    acc[3][1] = MFMA32(am[1], bm[3], acc[3][1]); D6; SB(); } while (0)
#define DW(e_, ch_, sl_, ii) dma.template w<ii>(c, e_, ch_, sl_)
#define DX(ch_, sl_, ii) dma.template x<ii>(c, ch_, sl_)
#define NOP (void)0
#define CH6(e_, ch_, sl_) DW(e_, ch_, sl_, 0), DW(e_, ch_, sl_, 1), DW(e_, ch_, sl_, 2), DW(e_, ch_, sl_, 3), DX(ch_, sl_, 0), DX(ch_, sl_, 1), DX(ch_, sl_, 2)
#define CH6C(cond, e_, ch_, sl_) if (cond) DW(e_, ch_, sl_, 0), if (cond) DW(e_, ch_, sl_, 1), if (cond) DW(e_, ch_, sl_, 2), if (cond) DW(e_, ch_, sl_, 3), if (cond) DX(ch_, sl_, 0), if (cond) DX(ch_, sl_, 1), if (cond) DX(ch_, sl_, 2)
#define KSTEP_(...) KSTEP(__VA_ARGS__)
#define KS(ar, br, slot, sp, am, bm) KSTEP(ar, br, slot, sp, am, bm, NOP, NOP, NOP, NOP, NOP, NOP, NOP)
#define CH7(e_, ch_, sl_) do { DW(e_, ch_, sl_, 0); DW(e_, ch_, sl_, 1); DW(e_, ch_, sl_, 2); DW(e_, ch_, sl_, 3); DX(ch_, sl_, 0); DX(ch_, sl_, 1); DX(ch_, sl_, 2); } while (0)

  auto zero = [&]() {
#pragma unroll
    for (int w = 0; w < 4; ++w) { acc[w][0] = f32x16{}; acc[w][1] = f32x16{}; bB[w] = bf16x8{}; }
    aB[0] = bf16x8{}; aB[1] = bf16x8{};
  };
  auto handoff = [&]() {
#pragma unroll
    for (int w = 0; w < 4; ++w) { acc[w][0] = MFMA32(aB[0], bB[w], acc[w][0]); acc[w][1] = MFMA32(aB[1], bB[w], acc[w][1]); }
#pragma unroll
    for (int w = 0; w < 4; ++w)
#pragma unroll
      for (int tt = 0; tt < 2; ++tt)
#pragma unroll
        for (int sp = 0; sp < 2; ++sp) {
          const f32x16& a = acc[w][tt];
          bf16x8 f = pack8(a[8 * sp], a[8 * sp + 1], a[8 * sp + 2], a[8 * sp + 3], a[8 * sp + 4], a[8 * sp + 5], a[8 * sp + 6], a[8 * sp + 7]);
          *(lds_bf16x8*)(lds + LDS_Q + (w * 12 + 4 * (j - 1) + 2 * tt + sp) * 1024 + lane * 16) = f;
        }
  };
  zero();
  WAIT_BAR(63);
  for (int p = 0; p < 10; ++p) {
    const int pr = (p + 1) % 3;
    lds_char* const lsave = lds;
    lds = lsave + (pr == 2 ? -LDS_WX : 2 * pr * WXS);
    KS(aA, bA, 0, 0, aB, bB);
    KS(aB, bB, 0, 1, aA, bA);
    KS(aA, bA, 1, 0, aB, bB);
    KS(aB, bB, 1, 1, aA, bA);
    lds = lsave;
    if (p == 9) CH7(1, 0, 0);
    WAIT_BAR(63);
  }
  for (int e = 1; e <= 4; ++e) {
    handoff();
    WAIT_BAR(63);
    WAIT_BAR(63);
    if (e == 4) { WAIT_BAR(63); break; }
    CH7(e, 1, 1); CH7(e, 2, 2); CH7(e, 3, 3);
    zero();
    WAIT_BAR(21);
    for (int mm = 0; mm < 5; ++mm) {
      const int c4 = 4 * mm;
      const bool lastm = (mm == 4);
      if (WXPOS == 0 && mm > 0) { CH7(e, c4 + 2, 2); CH7(e, c4 + 3, 3); }
      KS(aA, bA, 0, 0, aB, bB);
      KS(aB, bB, 0, 1, aA, bA);
      if (WXPOS == 1 && mm > 0) { CH7(e, c4 + 2, 2); CH7(e, c4 + 3, 3); }
      WAIT_BAR(14);
      if (WXPOS == 0 && !lastm) CH7(e, c4 + 4, 0);
      KS(aA, bA, 1, 0, aB, bB);
      KS(aB, bB, 1, 1, aA, bA);
      if (WXPOS == 1 && !lastm) CH7(e, c4 + 4, 0);
      if (lastm) { WAIT_BAR(0); } else { WAIT_BAR(7); }
      {
        const int e2 = lastm ? e + 1 : e, ch2 = lastm ? 0 : c4 + 5, sl2 = lastm ? 0 : 1;
        if (WXPOS == 0 && e2 < 4) CH7(e2, ch2, sl2);
        KS(aA, bA, 2, 0, aB, bB);
        KS(aB, bB, 2, 1, aA, bA);
        KS(aA, bA, 3, 0, aB, bB);
        KS(aB, bB, 3, 1, aA, bA);
        if (WXPOS == 1 && e2 < 4) CH7(e2, ch2, sl2);
        WAIT_BAR(7);
      }
    }
  }
  for (int i = 0; i < 15; ++i) { WAIT_BAR(63); }
#undef KSTEP
#undef KSTEP_
#undef CH6
#undef CH6C
#undef DX
#undef DW
#undef NOP
}

DI void q8_wave(const Ctx& c, lds_char* lds, int lane) {
  const unsigned lds0 = (unsigned)(uintptr_t)lds;
  const unsigned r16 = lane & 15, g = lane >> 4, q4 = (lane & 15) >> 2, p = lane & 3;
  unsigned wb[2][2];
#pragma unroll
  for (int s1 = 0; s1 < 2; ++s1)
#pragma unroll
    for (int a = 0; a < 2; ++a)
      wb[s1][a] = LDS_WX + 384u * (8u * g + 4u * a + q4) + 16u * (4u * (s1 ^ (q4 >> 1)) + (p ^ (a << 1)));
  const unsigned xb = LDS_WX + CHB + 64u * (128u + r16) + 16u * (g ^ ((r16 >> 2) & 3));
  unsigned kb[2];
#pragma unroll
  for (int s1 = 0; s1 < 2; ++s1) kb[s1] = LDS_K + 384u * r16 + 16u * ((4u * s1 + g) ^ ((r16 >> 1) & 7));
  unsigned vb[4];
#pragma unroll
  for (int T = 0; T < 4; ++T)
    vb[T] = LDS_V + 384u * (4u * g + q4) + 16u * (4u * ((T >> 1) ^ (q4 >> 1)) + 2u * ((T & 1) ^ (g & 1)) + (p >> 1)) + 8u * (p & 1);

  constexpr int TR = 32 / NBX, TL = 3 * TR;
  constexpr int TCH = (64 / TL) < 4 ? (64 / TL) : 4;
  unsigned toff;
  { const unsigned ll = lane < TL * TCH ? lane : TL * TCH - 1; toff = ((ll / TL) * 32u + TR * c.b + (ll % TL) / 3) * (HD * 2) + ((ll % TL) % 3) * 128u; }
  auto touchw = [&](int e, int ch) {
#pragma unroll
    for (int q = 0; q < 4 / TCH; ++q) touch4(c.wq + (size_t)(ch + q * TCH) * (32 * HD * 2) + e * (DH * 2), toff, lds0 + LDS_SCR);
  };
  constexpr int XS = 32 / NBX;
  constexpr int XR = SQ / XS;
  constexpr int XI = (2 * XR + 63) / 64;
  unsigned xtoff[XI];
#pragma unroll
  for (int q = 0; q < XI; ++q) { unsigned ll = q * 64 + lane; if (ll >= 2 * XR) ll = 2 * XR - 1; xtoff[q] = (XR * c.xs + (ll >> 1)) * (DM * 2) + (ll & 1) * 128u; }
  auto touchx0 = [&](int ch) {
#pragma unroll
    for (int q = 0; q < XI; ++q) touch4(c.xg + ch * 64, xtoff[q], lds0 + LDS_SCR);
  };
  auto touchx = [&](int ch) {
  };
#pragma unroll
  for (int g4 = 0; g4 < 5; ++g4) { touchw(0, 4 * g4); touchx0(4 * g4); }
  f32x4 qa[12], o[12];
  bf16x8 qf[6];
#pragma unroll
  for (int T = 0; T < 12; ++T) { o[T] = f32x4{}; qa[T] = f32x4{}; }
  float m = 0.f, mc = 0.f, l = 0.f, alpha = 1.f;
  bool resc = false;
  bf16x8 pf = {};
  bf16x8 fb[8] = {};
  f32x4 p0 = {}, p1 = {};
  int ks_rd = 0, vs_rd = 0;

#define KRD(i) ldsr(lds + ks_rd + kb[((i) >> 1) & 1] + 128 * ((i) >> 2) + 6144 * ((i) & 1))
#define VRD(T) tr2(lds + vs_rd + vb[(T) & 3] + 128 * ((T) >> 2), lds + vs_rd + vb[(T) & 3] + 128 * ((T) >> 2) + 6144)
#define WRD(slot, T) tr2(lds + (slot) * WXS + wb[((T) >> 1) & 1][0] + 128 * ((T) >> 2) + 8 * ((T) & 1), lds + (slot) * WXS + wb[((T) >> 1) & 1][1] + 128 * ((T) >> 2) + 8 * ((T) & 1))
#define EXP2M(x) __builtin_amdgcn_exp2f(__builtin_fmaf((x), QSCALE, mc))

  auto rescale = [&]() {
    asm volatile("" ::: "memory");
    if (__builtin_expect(resc, 0)) {
#pragma unroll
      for (int T = 0; T < 12; ++T)
#pragma unroll
        for (int r = 0; r < 4; ++r) o[T][r] *= alpha;
      resc = false;
    }
  };
  auto decide = [&](float mx) {
    asm volatile("" ::: "memory");
    if (__builtin_expect(__any(mx - m > THR / QSCALE), 0)) {
      float mn = __builtin_fmaxf(m, mx);
      alpha = __builtin_amdgcn_exp2f((m - mn) * QSCALE);
      m = mn; mc = -mn * QSCALE; l *= alpha; resc = true;
    }
  };
  auto chunk = [&](int slot) {
    bf16x8 b = ldsr(lds + slot * WXS + xb);
    bf16x8 a0 = WRD(slot, 0), a1 = WRD(slot, 1), a2 = WRD(slot, 2), a3 = WRD(slot, 3);
    SB();
    qa[0] = MFMA16(a0, b, qa[0]); a0 = WRD(slot, 4); SB();
    qa[1] = MFMA16(a1, b, qa[1]); a1 = WRD(slot, 5); SB();
    qa[2] = MFMA16(a2, b, qa[2]); a2 = WRD(slot, 6); SB();
    qa[3] = MFMA16(a3, b, qa[3]); a3 = WRD(slot, 7); SB();
    qa[4] = MFMA16(a0, b, qa[4]); a0 = WRD(slot, 8); SB();
    qa[5] = MFMA16(a1, b, qa[5]); a1 = WRD(slot, 9); SB();
    qa[6] = MFMA16(a2, b, qa[6]); a2 = WRD(slot, 10); SB();
    qa[7] = MFMA16(a3, b, qa[7]); a3 = WRD(slot, 11); SB();
    qa[8] = MFMA16(a0, b, qa[8]); qa[9] = MFMA16(a1, b, qa[9]); qa[10] = MFMA16(a2, b, qa[10]); qa[11] = MFMA16(a3, b, qa[11]); SB();
  };
  auto stepq = [&](auto nch_, auto sl_) {
    constexpr int NCH = decltype(nch_)::value, SL = decltype(sl_)::value;
    constexpr int NOPS = 24 + 12 * NCH, PV0 = 20 + 12 * NCH;
    f32x4 s0, s1; bf16x8 bx0, bx1; float mx;
    static_for<NOPS>([&](auto k_) {
      constexpr int k = decltype(k_)::value, b = k & 7;
      if constexpr (k < 8) { o[4 + k] = MFMA16(fb[b], pf, o[4 + k]); }
      else if constexpr (k < 20) {
        constexpr int i = k - 8;
        if constexpr (i == 0) s0 = MFMA16(fb[b], qf[0], (f32x4{}));
        else if constexpr (i == 1) s1 = MFMA16(fb[b], qf[0], (f32x4{}));
        else if constexpr (i & 1) s1 = MFMA16(fb[b], qf[i >> 1], s1);
        else s0 = MFMA16(fb[b], qf[i >> 1], s0);
      } else if constexpr (k < PV0) {
        constexpr int T = (k - 20) % 12, cc = (k - 20) / 12;
        qa[T] = MFMA16(fb[b], (cc ? bx1 : bx0), qa[T]);
      } else { o[k - PV0] = MFMA16(fb[b], pf, o[k - PV0]); }
      constexpr int n = k + 8;
      if constexpr (n < 20) fb[b] = KRD(n - 8);
      else if constexpr (n < PV0) fb[b] = WRD(SL + (n - 20) / 12, (n - 20) % 12);
      else if constexpr (n < NOPS) fb[b] = VRD(n - PV0);
      else fb[b] = VRD(4 + b);
      if constexpr (k == 2 && NCH >= 1) bx0 = ldsr(lds + SL * WXS + xb);
      if constexpr (k == 4 && NCH >= 2) bx1 = ldsr(lds + (SL + 1) * WXS + xb);
      if constexpr (k == 5) { float sum = (p0[0] + p0[1]) + (p0[2] + p0[3]) + (p1[0] + p1[1]) + (p1[2] + p1[3]); l += sum; PIN(l); }
      if constexpr (k == 9) { pf = pack8(p0[0], p0[1], p0[2], p0[3], p1[0], p1[1], p1[2], p1[3]); PIN(pf); }
      if constexpr (k == 12) { SB(); rescale(); }
      if constexpr (k == 21) {
        mx = max3(s0[0], s0[1], s0[2]);
        mx = max3(mx, s0[3], s1[0]); mx = max3(mx, s1[1], s1[2]); mx = __builtin_fmaxf(mx, s1[3]);
        mx = x16_max(xhalf_max(mx));
        SB(); decide(mx);
      }
      if constexpr (k == 22) { p0[0] = EXP2M(s0[0]); p0[1] = EXP2M(s0[1]); p0[2] = EXP2M(s0[2]); p0[3] = EXP2M(s0[3]); PIN(p0); }
      if constexpr (k == 23) { p1[0] = EXP2M(s1[0]); p1[1] = EXP2M(s1[1]); p1[2] = EXP2M(s1[2]); p1[3] = EXP2M(s1[3]); PIN(p1); }
      SB();
    });
    ks_rd = inc3(ks_rd); vs_rd = inc3(vs_rd);
  };
  auto newhead = [&]() {
#pragma unroll
    for (int s = 0; s < 6; ++s)
      qf[s] = pack8(qa[2 * s][0], qa[2 * s][1], qa[2 * s][2], qa[2 * s][3], qa[2 * s + 1][0], qa[2 * s + 1][1], qa[2 * s + 1][2], qa[2 * s + 1][3]);
#pragma unroll
    for (int T = 0; T < 12; ++T) qa[T] = f32x4{};
    f32x4 s0 = {}, s1 = {};
#pragma unroll
    for (int s = 0; s < 6; ++s) { bf16x8 a0 = KRD(2 * s), a1 = KRD(2 * s + 1); s0 = MFMA16(a0, qf[s], s0); s1 = MFMA16(a1, qf[s], s1); }
    float mx = max3(s0[0], s0[1], s0[2]);
    mx = max3(mx, s0[3], s1[0]); mx = max3(mx, s1[1], s1[2]); mx = __builtin_fmaxf(mx, s1[3]);
    m = x16_max(xhalf_max(mx)); mc = -m * QSCALE; l = 0.f; resc = false;
#pragma unroll
    for (int r = 0; r < 4; ++r) { p0[r] = EXP2M(s0[r]); p1[r] = EXP2M(s1[r]); }
    PIN(p0); PIN(p1);
    ks_rd = inc3(ks_rd);
  };

  using I0 = std::integral_constant<int, 0>; using I1 = std::integral_constant<int, 1>; using I2 = std::integral_constant<int, 2>;
  auto finish = [&](int e) {
#pragma unroll
    for (int k = 0; k < 8; ++k) o[4 + k] = MFMA16(fb[k], pf, o[4 + k]);
    rescale();
    l += (p0[0] + p0[1]) + (p0[2] + p0[3]) + (p1[0] + p1[1]) + (p1[2] + p1[3]);
    pf = pack8(p0[0], p0[1], p0[2], p0[3], p1[0], p1[1], p1[2], p1[3]);
#pragma unroll
    for (int T = 0; T < 12; ++T) { bf16x8 a = VRD(T); o[T] = MFMA16(a, pf, o[T]); }
    vs_rd = inc3(vs_rd);
    const float inv = 1.0f / x16_sum(xhalf_sum(l));
    char* op = c.og + (size_t)4 * 384 * 1024 + ((size_t)((c.h0 + e) * 6) * 64 + lane) * 16;
#pragma unroll
    for (int s = 0; s < 6; ++s) {
      bf16x8 f = pack8(o[2 * s][0] * inv, o[2 * s][1] * inv, o[2 * s][2] * inv, o[2 * s][3] * inv,
                       o[2 * s + 1][0] * inv, o[2 * s + 1][1] * inv, o[2 * s + 1][2] * inv, o[2 * s + 1][3] * inv);
      OSTORE(f, op + s * 1024);
      o[2 * s] = f32x4{}; o[2 * s + 1] = f32x4{};
    }
#pragma unroll
    for (int k = 0; k < 8; ++k) fb[k] = bf16x8{};
  };
  WAIT_BAR(63);
  for (int p = 0; p < 10; ++p) {
    const int pr = (p + 1) % 3;
    if (p == 4) touchw(1, 0);
    if (p == 7) touchw(1, 4);
    lds_char* const lsave = lds;
    lds = lsave + (pr == 2 ? -LDS_WX : 2 * pr * WXS);
    chunk(0); chunk(1);
    lds = lsave;
    WAIT_BAR(63);
  }
  WAIT_BAR(63);
  WAIT_BAR(63);
  newhead();
  WAIT_BAR(63);
  for (int e = 0; e < 3; ++e) {
    for (int mm = 0; mm < 5; ++mm) {
      { const int g4 = mm + TOUCH_AHEAD; if (g4 < 5) { touchw(e + 1, 4 * g4); touchx(4 * g4); } else if (e < 2) { touchw(e + 2, 4 * (g4 - 5)); touchx(4 * (g4 - 5)); } }
      stepq(I1{}, I0{}); WAIT_BAR(63);
      stepq(I1{}, I1{}); WAIT_BAR(63);
      stepq(I2{}, I2{}); WAIT_BAR(63);
    }
    finish(e);
    WAIT_BAR(63);
    WAIT_BAR(63);
    newhead();
    WAIT_BAR(63);
  }
  for (int i = 0; i < 15; ++i) { stepq(I0{}, I0{}); WAIT_BAR(63); }
  finish(3);
#undef KRD
#undef VRD
#undef WRD
#undef EXP2M
}

__global__ __launch_bounds__(512, 2) void attn_fused(const bf16* xA, const bf16* kA, const bf16* vA, const bf16* wqA,
                                                     const bf16* xB, const bf16* kB, const bf16* vB, const bf16* wqB, char* ows) {
  extern __shared__ __attribute__((aligned(16))) char smem[];
  lds_char* lds = (lds_char*)smem;
  const int g = blockIdx.x, xcd = g & 7, jj = g >> 3;
  const int br = xcd >> 2, bl = jj & 7, b = 8 * ((xcd >> 1) & 1) + bl, h0 = 4 * (4 * (xcd & 1) + (jj >> 3));
  const int wave = __builtin_amdgcn_readfirstlane(threadIdx.x >> 6), lane = threadIdx.x & 63;
  Ctx c;
  const size_t kvo = (size_t)(b * NH + h0) * SK * DH * 2;
  c.kg = (const char*)(br ? kB : kA) + kvo;
  c.vg = (const char*)(br ? vB : vA) + kvo;
  c.wq = (const char*)(br ? wqB : wqA) + h0 * DH * 2;
  c.xg = (const char*)(br ? xB : xA) + (size_t)b * SQ * DM * 2;
  c.og = ows + (size_t)(br * NB + b) * O_PER_B;
  c.h0 = h0; c.b = bl; c.xs = jj / NBX;
  if (threadIdx.x == 0) *(unsigned*)(ows + (size_t)2 * NB * O_PER_B + g * 128) = 0u;
  if (wave < 4) main_wave(c, lds, wave, lane);
  else if (wave == 4) {
    __builtin_amdgcn_s_setprio(2);
    q8_wave(c, lds, lane); }
  else proj_wave(c, lds, wave - 4, lane);
}

constexpr int P_WOB = 32 * 320;
constexpr int P_OB = 9 * 1024;
constexpr int P_CH = P_WOB + P_OB;
constexpr int P_STEP = 2 * P_CH;
constexpr int P_NSLOT = 4, P_LDS = P_NSLOT * P_STEP;
constexpr int P_NSTEP = 48;
constexpr size_t WS_O_BYTES = (size_t)2 * NB * O_PER_B;
constexpr size_t WS_FLAG = WS_O_BYTES;
constexpr size_t WS_XCH = WS_FLAG + 256 * 128;
constexpr int XCH_BYTES = 49152;

DI void st_sc1(char* p, f32x4 v) { asm volatile("global_store_dwordx4 %0, %1, off sc1\n\ts_nop 1" :: "v"(p), "v"(v) : "memory"); }

__global__ __launch_bounds__(512, 2) void oproj(const char* ows, const bf16* woA, const bf16* woB, bf16* out, char* wsx) {
  extern __shared__ __attribute__((aligned(16))) char smem[];
  lds_char* lds = (lds_char*)smem;
  const unsigned lds0 = (unsigned)(uintptr_t)lds;
  const int g = blockIdx.x, xcd = g & 7, jj = g >> 3;
  const int br = xcd >> 2, kh = (xcd >> 1) & 1, b = 8 * (xcd & 1) + (jj & 7), nq = jj >> 3;
  const int wave = __builtin_amdgcn_readfirstlane(threadIdx.x >> 6), lane = threadIdx.x & 63;
  const char* og = ows + (size_t)(br * NB + b) * O_PER_B;
  const char* wo = (const char*)(br ? woB : woA) + (size_t)kh * 3072 * (DM * 2) + nq * 320;
  const int partner = g ^ 2;
  char* xs = wsx + (WS_XCH - WS_FLAG) + (size_t)g * XCH_BYTES;
  const char* xr = wsx + (WS_XCH - WS_FLAG) + (size_t)partner * XCH_BYTES;
  unsigned* myflag = (unsigned*)(wsx + g * 128);
  unsigned* pflag = (unsigned*)(wsx + partner * 128);
  bf16* orow0 = out + (size_t)(b * SQ) * (2 * DM) + br * DM + nq * 160;
#define P_BAR(N) do { SB(); asm volatile("s_waitcnt vmcnt(" #N ") lgkmcnt(0)\n\ts_barrier" ::: "memory"); SB(); } while (0)

  if (wave < 4) {
    const int w = wave, h2 = lane >> 5;
    const unsigned blk = (lane >> 4) & 1, q4 = (lane & 15) >> 2, p = lane & 3;
    const unsigned ab = 320u * (4u * h2 + q4) + 8u * (4u * (p & 1) + 2u * blk + (p >> 1));
    const unsigned bb = P_WOB + 2048u * w + lane * 16;
    f32x16 acc[5];
#pragma unroll
    for (int t = 0; t < 5; ++t) acc[t] = f32x16{};
    bf16x8 aA[5] = {}, aB[5] = {}, bA = {}, bB = {};
#define P_KS(ar, br_, off, s, am, bm) do { \
      const lds_char* pa_ = lds + (off) + ab + 5120 * (s); \
      acc[0] = MFMA32(am[0], bm, acc[0]); br_ = ldsr(lds + (off) + bb + 1024 * (s)); ar[0] = tr2(pa_, pa_ + 2560); SB(); \
      acc[1] = MFMA32(am[1], bm, acc[1]); ar[1] = tr2(pa_ + 64, pa_ + 2560 + 64); SB(); \
      acc[2] = MFMA32(am[2], bm, acc[2]); ar[2] = tr2(pa_ + 128, pa_ + 2560 + 128); SB(); \
      acc[3] = MFMA32(am[3], bm, acc[3]); ar[3] = tr2(pa_ + 192, pa_ + 2560 + 192); SB(); \
      acc[4] = MFMA32(am[4], bm, acc[4]); ar[4] = tr2(pa_ + 256, pa_ + 2560 + 256); SB(); } while (0)
    P_BAR(63);
    int so = 0;
    for (int st = 0; st < P_NSTEP; ++st) {
      P_KS(aA, bA, so, 0, aB, bB);
      P_KS(aB, bB, so, 1, aA, bA);
      P_KS(aA, bA, so + P_CH, 0, aB, bB);
      P_KS(aB, bB, so + P_CH, 1, aA, bA);
      so = (so == (P_NSLOT - 1) * P_STEP) ? 0 : so + P_STEP;
      P_BAR(63);
    }
#pragma unroll
    for (int t = 0; t < 5; ++t) acc[t] = MFMA32(aB[t], bB, acc[t]);
#undef P_KS
    const bool keep = ((w >> 1) == kh);
    const unsigned xo = (w & 1) * 20480u + lane * 16;
    if (!keep) {
#pragma unroll
      for (int t = 0; t < 5; ++t)
#pragma unroll
        for (int r = 0; r < 4; ++r) { f32x4 v = {acc[t][4 * r], acc[t][4 * r + 1], acc[t][4 * r + 2], acc[t][4 * r + 3]}; st_sc1(xs + xo + (t * 4 + r) * 1024, v); }
    }
    P_BAR(0);
    if (w == 0) {
      if (lane == 0) __hip_atomic_store(myflag, 1u, __ATOMIC_RELAXED, __HIP_MEMORY_SCOPE_AGENT);
      while (__hip_atomic_load(pflag, __ATOMIC_RELAXED, __HIP_MEMORY_SCOPE_AGENT) == 0u) __builtin_amdgcn_s_sleep(2);
      __builtin_amdgcn_fence(__ATOMIC_ACQUIRE, "agent");
    }
    P_BAR(0);
    if (keep) {
      bf16* orow = orow0 + (size_t)(32 * w + (lane & 31)) * (2 * DM) + 16 * h2;
#pragma unroll
      for (int t = 0; t < 5; ++t) {
        f32x4 v[4];
#pragma unroll
        for (int r = 0; r < 4; ++r) v[r] = __builtin_nontemporal_load((const f32x4*)(xr + xo + (t * 4 + r) * 1024));
#pragma unroll
        for (int hh = 0; hh < 2; ++hh) {
          bf16x8 f = pack8(acc[t][8 * hh] + v[2 * hh][0], acc[t][8 * hh + 1] + v[2 * hh][1], acc[t][8 * hh + 2] + v[2 * hh][2], acc[t][8 * hh + 3] + v[2 * hh][3],
                           acc[t][8 * hh + 4] + v[2 * hh + 1][0], acc[t][8 * hh + 5] + v[2 * hh + 1][1], acc[t][8 * hh + 6] + v[2 * hh + 1][2], acc[t][8 * hh + 7] + v[2 * hh + 1][3]);
          *(bf16x8*)(orow + 32 * t + 8 * hh) = f;
        }
      }
    }
  } else {
    const int j = wave - 4;
    const int T0 = j < 2 ? 3 * j : 2 * j + 2;
    const bool three = j < 2;
    const unsigned gq = lane >> 4, q4 = (lane & 15) >> 2, p = lane & 3;
    const unsigned ab = 320u * (4u * gq + q4) + 8u * p + 32u * T0;
    const unsigned bb = P_WOB + 8192u + lane * 16;
    unsigned wofs[3];
#pragma unroll
    for (int ii = 0; ii < 3; ++ii) { unsigned pp = 64u * (j + 4 * ii) + lane; unsigned row = pp / 20; wofs[ii] = row * (DM * 2) + (pp - 20 * row) * 16; }
    const unsigned l16 = lane * 16;
    const char* wp = wo;
    const char* op = og + (size_t)(j * 384 + kh * 192) * 1024;
    const char* o8 = og + (size_t)4 * 384 * 1024 + (size_t)(kh * 96) * 1024;
    auto issue = [&](unsigned d) {
      dma16<0, 0>(wp, wofs[0], d + 1024u * j); dma16<0, 4096>(wp, wofs[1], d + 1024u * j);
      if (j < 2) dma16<0, 8192>(wp, wofs[2], d + 1024u * j);
      dma16<0, P_WOB>(op, l16, d + 2048u * j); dma16<0, P_WOB + 1024>(op + 1024, l16, d + 2048u * j);
      if (j == 2) dma16<0, P_WOB + 8192>(o8, l16, d);
      wp += 32 * DM * 2; op += 2048; o8 += 1024;
    };
    f32x4 acc[3] = {};
#pragma unroll
    for (int s = 0; s < 3; ++s) { issue(lds0 + s * P_STEP); issue(lds0 + s * P_STEP + P_CH); }
    P_BAR(16);
    int so = 0, sw = 3 * P_STEP;
    for (int st = 0; st < P_NSTEP; ++st) {
#pragma unroll
      for (int cc = 0; cc < 2; ++cc) {
        const lds_char* pa = lds + so + cc * P_CH + ab;
        bf16x8 bq = ldsr(lds + so + cc * P_CH + bb);
        bf16x8 a0 = tr2(pa, pa + 5120), a1 = tr2(pa + 32, pa + 5120 + 32);
        acc[0] = MFMA16(a0, bq, acc[0]); acc[1] = MFMA16(a1, bq, acc[1]);
        if (three) { bf16x8 a2 = tr2(pa + 64, pa + 5120 + 64); acc[2] = MFMA16(a2, bq, acc[2]); }
      }
      if (st + 3 < P_NSTEP) { issue(lds0 + sw); issue(lds0 + sw + P_CH); }
      so = (so == (P_NSLOT - 1) * P_STEP) ? 0 : so + P_STEP;
      sw = (sw == (P_NSLOT - 1) * P_STEP) ? 0 : sw + P_STEP;
      if (st + 3 < P_NSTEP) { P_BAR(16); } else if (st + 3 == P_NSTEP) { P_BAR(8); } else { P_BAR(0); }
    }
    const bool keep = ((j >> 1) == kh);
    const unsigned xo = 40960u + (j & 1) * 3072u + lane * 16;
    if (!keep) {
#pragma unroll
      for (int t = 0; t < 3; ++t) st_sc1(xs + xo + t * 1024, acc[t]);
    }
    P_BAR(0);
    P_BAR(0);
    if (keep) {
      bf16* orow = orow0 + (size_t)(128 + (lane & 15)) * (2 * DM) + 16 * T0 + 4 * gq;
#pragma unroll
      for (int t = 0; t < 3; ++t) {
        if (t < 2 || three) {
          f32x4 v = __builtin_nontemporal_load((const f32x4*)(xr + xo + t * 1024));
          uint2 f = {cvtpk(acc[t][0] + v[0], acc[t][1] + v[1]), cvtpk(acc[t][2] + v[2], acc[t][3] + v[3])};
          *(uint2*)(orow + 16 * t) = f;
        }
      }
    }
  }
#undef P_BAR
}

extern "C" void kernel_launch(void* const* d_in, const int* in_sizes, int n_in, void* d_out, int out_size, void* d_ws, size_t ws_size,
                              hipStream_t stream) {
  (void)hipFuncSetAttribute((const void*)attn_fused, hipFuncAttributeMaxDynamicSharedMemorySize, LDS_TOTAL + 3840);
  (void)hipFuncSetAttribute((const void*)oproj, hipFuncAttributeMaxDynamicSharedMemorySize, P_LDS);
  attn_fused<<<256, 512, LDS_TOTAL + 3840, stream>>>((const bf16*)d_in[0], (const bf16*)d_in[1], (const bf16*)d_in[2], (const bf16*)d_in[3],
                                              (const bf16*)d_in[5], (const bf16*)d_in[6], (const bf16*)d_in[7], (const bf16*)d_in[8], (char*)d_ws);
  oproj<<<256, 512, P_LDS, stream>>>((const char*)d_ws, (const bf16*)d_in[4], (const bf16*)d_in[9], (bf16*)d_out, (char*)d_ws + WS_FLAG);
}
```

```cpp
#include <hip/hip_runtime.h>
#include <hip/hip_bf16.h>
#include <stdint.h>
#include <utility>
#include <type_traits>

typedef __hip_bfloat16 bf16;
typedef short bf16x8 __attribute__((ext_vector_type(8)));
typedef short s16x4 __attribute__((ext_vector_type(4)));
typedef float f32x16 __attribute__((ext_vector_type(16)));
typedef float f32x4 __attribute__((ext_vector_type(4)));
typedef float f32x2 __attribute__((ext_vector_type(2)));
typedef unsigned u32x4 __attribute__((ext_vector_type(4)));
typedef __bf16 bf2_t __attribute__((ext_vector_type(2)));
typedef __attribute__((address_space(3))) char lds_char;
typedef __attribute__((address_space(3))) s16x4 lds_s16x4;
typedef __attribute__((address_space(3))) bf16x8 lds_bf16x8;

#define DI __device__ __forceinline__
#define MFMA32(a, b, c) __builtin_amdgcn_mfma_f32_32x32x16_bf16((a), (b), (c), 0, 0, 0)
#define MFMA16(a, b, c) __builtin_amdgcn_mfma_f32_16x16x32_bf16((a), (b), (c), 0, 0, 0)
#define SB() __builtin_amdgcn_sched_barrier(0)
#define PIN(x) asm volatile("" : "+v"(x))

constexpr int NB = 16, SQ = 144, DM = 640, NH = 32, SK = 512, DH = 192, HD = NH * DH;
constexpr int CHB = 12288;
constexpr int XB = 9216;
constexpr int LDS_K = 0, LDS_V = 3 * CHB, LDS_WX = 6 * CHB, WXS = CHB + XB;
constexpr int LDS_Q = LDS_WX + WXS;
constexpr int LDS_SCR = LDS_WX + 4 * WXS;
constexpr int LDS_TOTAL = LDS_SCR + 256;
constexpr float QSCALE = 0.07216878364870322f * 1.4426950408889634f;
#define KVPOS 2
#define WXPOS 0
#define NBX 8
#define TOUCH_AHEAD 2
constexpr float THR = 8.0f;
constexpr size_t O_PER_B = (size_t)SQ * HD * 2;

DI unsigned cvtpk(float lo, float hi) {
  f32x2 v = {lo, hi};
  bf2_t b = __builtin_convertvector(v, bf2_t);
  return __builtin_bit_cast(unsigned, b);
}
DI bf16x8 pack8(float a0, float a1, float a2, float a3, float a4, float a5, float a6, float a7) {
  u32x4 w = {cvtpk(a0, a1), cvtpk(a2, a3), cvtpk(a4, a5), cvtpk(a6, a7)};
  return __builtin_bit_cast(bf16x8, w);
}
#define OSTORE(v, p) __builtin_nontemporal_store((v), (bf16x8*)(p))
DI s16x4 trread(const lds_char* p) { return __builtin_amdgcn_ds_read_tr16_b64_v4i16((lds_s16x4*)p); }
DI bf16x8 tr2(const lds_char* p0, const lds_char* p1) {
  s16x4 lo = trread(p0), hi = trread(p1);
  return __builtin_shufflevector(lo, hi, 0, 1, 2, 3, 4, 5, 6, 7);
}
DI bf16x8 ldsr(const lds_char* p) { return *(const lds_bf16x8*)p; }

template <int NT, int IMM = 0>
DI void dma16(const char* sbase, unsigned voff, unsigned ldsdst) {
  unsigned keep;
  if constexpr (NT)
    asm volatile("s_mov_b32 %0, m0\n\ts_add_u32 m0, %3, %4\n\ts_nop 0\n\tglobal_load_lds_dwordx4 %1, %2 nt\n\ts_mov_b32 m0, %0"
                 : "=&s"(keep) : "v"(voff), "s"(sbase), "s"(ldsdst), "i"(IMM) : "memory", "scc");
  else
    asm volatile("s_mov_b32 %0, m0\n\ts_add_u32 m0, %3, %4\n\ts_nop 0\n\tglobal_load_lds_dwordx4 %1, %2\n\ts_mov_b32 m0, %0"
                 : "=&s"(keep) : "v"(voff), "s"(sbase), "s"(ldsdst), "i"(IMM) : "memory", "scc");
}
DI void touch4(const char* sbase, unsigned voff, unsigned ldsdst) {
  unsigned keep;
  asm volatile("s_mov_b32 %0, m0\n\ts_mov_b32 m0, %3\n\ts_nop 0\n\tglobal_load_lds_dword %1, %2\n\ts_mov_b32 m0, %0"
               : "=&s"(keep) : "v"(voff), "s"(sbase), "s"(ldsdst) : "memory");
}
#define WAIT_BAR(N) do { SB(); asm volatile("s_waitcnt vmcnt(" #N ") lgkmcnt(0)\n\ts_barrier" ::: "memory"); SB(); } while (0)

DI unsigned swv(unsigned row) { return (((row >> 1) & 1) << 2) | (((row >> 2) & 1) << 1); }
DI float max3(float a, float b, float c) { return __builtin_fmaxf(__builtin_fmaxf(a, b), c); }
DI float xhalf_max(float m) {
  auto rr = __builtin_amdgcn_permlane32_swap(__float_as_uint(m), __float_as_uint(m), false, false);
  return __builtin_fmaxf(__uint_as_float(rr[0]), __uint_as_float(rr[1]));
}
DI float xhalf_sum(float m) {
  auto rr = __builtin_amdgcn_permlane32_swap(__float_as_uint(m), __float_as_uint(m), false, false);
  return __uint_as_float(rr[0]) + __uint_as_float(rr[1]);
}
DI float x16_max(float m) {
  auto rr = __builtin_amdgcn_permlane16_swap(__float_as_uint(m), __float_as_uint(m), false, false);
  return __builtin_fmaxf(__uint_as_float(rr[0]), __uint_as_float(rr[1]));
}
DI float x16_sum(float m) {
  auto rr = __builtin_amdgcn_permlane16_swap(__float_as_uint(m), __float_as_uint(m), false, false);
  return __uint_as_float(rr[0]) + __uint_as_float(rr[1]);
}
template <int N, class F, int... Is> DI void static_for_impl(F&& f, std::integer_sequence<int, Is...>) { (f(std::integral_constant<int, Is>{}), ...); }
template <int N, class F> DI void static_for(F&& f) { static_for_impl<N>(f, std::make_integer_sequence<int, N>{}); }
DI int inc3(int x) { return x == 2 * CHB ? 0 : x + CHB; }

struct Ctx {
  const char* kg;
  const char* vg;
  const char* wq;
  const char* xg;
  char* og;
  int h0;
  int b, xs;
};

DI void main_wave(const Ctx& c, lds_char* lds, int w, int lane) {
  const unsigned lds0 = (unsigned)(uintptr_t)lds;
  const int r32 = lane & 31, h2 = lane >> 5;
  const int wi = w > 0 ? w - 1 : 0;
  const bool dm = w > 0;
  unsigned koff[2], voff;
#pragma unroll
  for (int ii = 0; ii < 2; ++ii) {
    unsigned p = 64u * (2 * wi + ii) + lane, row = (p >> 3) / 3;
    koff[ii] = 16u * (p ^ ((row >> 1) & 7));
  }
  { unsigned p = 64u * wi + lane, row = (p >> 3) / 3; voff = 16u * (p ^ swv(row)); }
  const unsigned dk = lds0 + LDS_K + 2048u * wi, dv = lds0 + LDS_V + 1024u * wi;
  unsigned kb[4];
#pragma unroll
  for (int s = 0; s < 4; ++s) kb[s] = LDS_K + 384u * r32 + 16u * ((2u * s + h2) ^ ((r32 >> 1) & 7));
  unsigned vb[2];
  {
    const unsigned blk = (lane >> 4) & 1, q4 = (lane & 15) >> 2, p = lane & 3;
#pragma unroll
    for (int t = 0; t < 2; ++t)
      vb[t] = LDS_V + 384u * (4u * h2 + q4) + 16u * (4u * (t ^ (q4 >> 1)) + 2u * (blk ^ h2) + (p >> 1)) + 8u * (p & 1);
  }

  bf16x8 qf[12];
  f32x16 o[6];
#pragma unroll
  for (int t = 0; t < 6; ++t) o[t] = f32x16{};
  float m = 0.f, mc = 0.f, l = 0.f, alpha = 1.f;
  bool resc = false;
  bf16x8 pf0 = {}, pf1 = {};
  bf16x8 f0 = {}, f1 = {}, f2 = {}, f3 = {}, f4 = {}, f5 = {};
  f32x16 sA = {}, sB = {};
  int ks_rd = 0;
  int vs_rd = 0;
  const char* kptr = c.kg;
  const char* vptr = c.vg;
  int ks_wr = 0, vs_wr = 0;

#define KRD(ks) ldsr(lds + ks_rd + kb[(ks) & 3] + 128 * ((ks) >> 2))
#define VRD(j) tr2(lds + vs_rd + vb[((j) % 6) & 1] + 128 * (((j) % 6) >> 1) + 6144 * ((j) / 6), lds + vs_rd + vb[((j) % 6) & 1] + 128 * (((j) % 6) >> 1) + 6144 * ((j) / 6) + 3072)
#define DMAK4() do { if (dm) { dma16<1, 0>(kptr, koff[0], dk + ks_wr); dma16<1, 1024>(kptr, koff[1], dk + ks_wr); \
    dma16<1, 6144>(kptr + 6144, koff[0], dk + ks_wr); dma16<1, 7168>(kptr + 6144, koff[1], dk + ks_wr); } } while (0)
#define DMAV4P(vp_, d_) do { if (dm) { dma16<1, 0>(vp_, voff, d_); dma16<1, 3072>((vp_) + 3072, voff, d_); \
    dma16<1, 6144>((vp_) + 6144, voff, d_); dma16<1, 9216>((vp_) + 9216, voff, d_); } } while (0)
#define DMAV4() DMAV4P(vptr, dv + vs_wr)
#define ADVK() do { kptr += CHB; ks_wr = inc3(ks_wr); } while (0)
#define ADVV() do { vptr += CHB; vs_wr = inc3(vs_wr); } while (0)
#define EXP2M(x) __builtin_amdgcn_exp2f(__builtin_fmaf((x), QSCALE, mc))

  auto rescale = [&]() {
    asm volatile("" ::: "memory");
    if (__builtin_expect(resc, 0)) {
#pragma unroll
      for (int tt = 0; tt < 6; ++tt)
#pragma unroll
        for (int r = 0; r < 16; ++r) o[tt][r] *= alpha;
      resc = false;
    }
  };
  auto decide = [&](float mx) {
    asm volatile("" ::: "memory");
    if (__builtin_expect(__any(mx - m > THR / QSCALE), 0)) {
      float mn = __builtin_fmaxf(m, mx);
      alpha = __builtin_amdgcn_exp2f((m - mn) * QSCALE);
      m = mn; mc = -mn * QSCALE; l *= alpha; resc = true;
    }
  };
  auto step = [&](f32x16& sN, f32x16& sP, bool gk, bool gv) {
    o[0] = MFMA32(f0, pf1, o[0]); f0 = KRD(0); sP[6] = EXP2M(sP[6]); sP[7] = EXP2M(sP[7]); PIN(sP); SB();
    o[1] = MFMA32(f1, pf1, o[1]); f1 = KRD(1); sP[8] = EXP2M(sP[8]); sP[9] = EXP2M(sP[9]); PIN(sP); SB();
    o[2] = MFMA32(f2, pf1, o[2]); f2 = KRD(2); sP[10] = EXP2M(sP[10]); sP[11] = EXP2M(sP[11]); PIN(sP); SB();
    o[3] = MFMA32(f3, pf1, o[3]); f3 = KRD(3); sP[12] = EXP2M(sP[12]); sP[13] = EXP2M(sP[13]); PIN(sP); SB();
    o[4] = MFMA32(f4, pf1, o[4]); f4 = KRD(4); sP[14] = EXP2M(sP[14]); sP[15] = EXP2M(sP[15]); PIN(sP); SB();
    o[5] = MFMA32(f5, pf1, o[5]); f5 = KRD(5); pf0 = pack8(sP[0], sP[1], sP[2], sP[3], sP[4], sP[5], sP[6], sP[7]); PIN(pf0); SB();
    float sum;
    sN = MFMA32(f0, qf[0], (f32x16{})); f0 = KRD(6); sum = sP[0] + sP[1]; sum += sP[2]; sum += sP[3]; PIN(sum); SB();
    sN = MFMA32(f1, qf[1], sN); f1 = KRD(7); pf1 = pack8(sP[8], sP[9], sP[10], sP[11], sP[12], sP[13], sP[14], sP[15]); PIN(pf1); SB();
    sN = MFMA32(f2, qf[2], sN); f2 = KRD(8); sum += sP[4]; sum += sP[5]; sum += sP[6]; sum += sP[7]; PIN(sum); SB();
    sN = MFMA32(f3, qf[3], sN); f3 = KRD(9); sum += sP[8]; sum += sP[9]; sum += sP[10]; sum += sP[11]; PIN(sum); SB();
    sN = MFMA32(f4, qf[4], sN); f4 = KRD(10); SB();
    sN = MFMA32(f5, qf[5], sN); f5 = KRD(11); sum += sP[12]; sum += sP[13]; sum += sP[14]; sum += sP[15]; l += sum; PIN(l); SB();
    rescale();
    SB();
    sN = MFMA32(f0, qf[6], sN); f0 = VRD(0); SB();
    sN = MFMA32(f1, qf[7], sN); f1 = VRD(1); SB();
    sN = MFMA32(f2, qf[8], sN); f2 = VRD(2); SB();
    sN = MFMA32(f3, qf[9], sN); f3 = VRD(3); SB();
    sN = MFMA32(f4, qf[10], sN); f4 = VRD(4); SB();
    sN = MFMA32(f5, qf[11], sN); f5 = VRD(5); SB();
    o[0] = MFMA32(f0, pf0, o[0]); f0 = VRD(6); SB();
    o[1] = MFMA32(f1, pf0, o[1]); f1 = VRD(7); SB();
    float mx = max3(sN[0], sN[1], sN[2]), mx2 = max3(sN[3], sN[4], sN[5]);
    mx = max3(mx, sN[6], sN[7]); mx2 = max3(mx2, sN[8], sN[9]);
    o[2] = MFMA32(f2, pf0, o[2]); f2 = VRD(8);
    mx = max3(mx, sN[10], sN[11]); mx2 = max3(mx2, sN[12], sN[13]); mx = max3(mx, sN[14], sN[15]); mx = __builtin_fmaxf(mx, mx2);
    mx = xhalf_max(mx);
    SB();
    decide(mx);
    SB();
    o[3] = MFMA32(f3, pf0, o[3]); f3 = VRD(9); sN[0] = EXP2M(sN[0]); sN[1] = EXP2M(sN[1]); PIN(sN); SB();
    o[4] = MFMA32(f4, pf0, o[4]); f4 = VRD(10); sN[2] = EXP2M(sN[2]); sN[3] = EXP2M(sN[3]); PIN(sN); SB();
    o[5] = MFMA32(f5, pf0, o[5]); f5 = VRD(11); sN[4] = EXP2M(sN[4]); sN[5] = EXP2M(sN[5]); PIN(sN); SB();
    if (gk) { DMAK4(); }
    if (gv) { DMAV4(); }
    if (gk) ADVK();
    if (gv) ADVV();
    ks_rd = inc3(ks_rd); vs_rd = inc3(vs_rd);
  };
  auto readq = [&]() {
    unsigned qb = LDS_Q + w * 12288 + lane * 16;
    PIN(qb);
#pragma unroll
    for (int ks = 0; ks < 12; ++ks) qf[ks] = ldsr(lds + qb + ks * 1024);
  };
  auto newhead = [&](auto epi_, float inv, char* op) {
    constexpr bool EPI = decltype(epi_)::value;
    f0 = KRD(0); f1 = KRD(1); f2 = KRD(2); f3 = KRD(3); f4 = KRD(4); f5 = KRD(5);
    SB();
    static_for<12>([&](auto k_) {
      constexpr int k = decltype(k_)::value;
      bf16x8& f = (k % 6 == 0) ? f0 : (k % 6 == 1) ? f1 : (k % 6 == 2) ? f2 : (k % 6 == 3) ? f3 : (k % 6 == 4) ? f4 : f5;
      if constexpr (k == 0) sA = MFMA32(f, qf[0], (f32x16{})); else sA = MFMA32(f, qf[k], sA);
      if constexpr (k < 6) f = KRD(k + 6); else f = bf16x8{};
      if constexpr (EPI) {
        constexpr int tt = k >> 1, sp = k & 1;
        bf16x8 v = pack8(o[tt][8 * sp] * inv, o[tt][8 * sp + 1] * inv, o[tt][8 * sp + 2] * inv, o[tt][8 * sp + 3] * inv,
                         o[tt][8 * sp + 4] * inv, o[tt][8 * sp + 5] * inv, o[tt][8 * sp + 6] * inv, o[tt][8 * sp + 7] * inv);
        OSTORE(v, (op + k * 1024) + (unsigned)(lane * 16));
        if constexpr (sp == 1) { o[tt] = f32x16{}; PIN(o[tt]); }
      }
      SB();
    });
    float mx = max3(sA[0], sA[1], sA[2]);
    mx = max3(mx, sA[3], sA[4]); mx = max3(mx, sA[5], sA[6]); mx = max3(mx, sA[7], sA[8]); mx = max3(mx, sA[9], sA[10]);
    mx = max3(mx, sA[11], sA[12]); mx = max3(mx, sA[13], sA[14]); mx = __builtin_fmaxf(mx, sA[15]);
    m = xhalf_max(mx); mc = -m * QSCALE; l = 0.f; resc = false;
#pragma unroll
    for (int r = 0; r < 6; ++r) sA[r] = EXP2M(sA[r]);
    PIN(sA);
    ks_rd = inc3(ks_rd);
  };

  {
    unsigned wo[3], xo[3];
#pragma unroll
    for (int ii = 0; ii < 3; ++ii) {
      unsigned pp = 64u * (3 * w + ii) + lane, row = (pp >> 3) / 3, c16 = (pp ^ swv(row)) - 24u * row;
      wo[ii] = row * (HD * 2) + 16u * c16;
      unsigned r = 16u * (w + 4 * ii) + (lane >> 2);
      xo[ii] = r * (DM * 2) + 16u * ((lane & 3) ^ ((r >> 2) & 3));
    }
    auto pair_issue = [&](int p) {
      const int pr = (p + 1) % 3;
      const unsigned base = lds0 + (pr == 2 ? 0 : LDS_WX + 2 * pr * WXS);
      const unsigned bw = base + 3072u * w, bx = base + CHB + 1024u * w;
      const char* sw = c.wq + (size_t)p * (64 * HD * 2);
      const char* sx = c.xg + p * 128;
      dma16<0, 0>(sw, wo[0], bw); dma16<0, 1024>(sw, wo[1], bw); dma16<0, 2048>(sw, wo[2], bw);
      dma16<0, 0>(sx, xo[0], bx); dma16<0, 4096>(sx, xo[1], bx); if (w == 0) dma16<0, 8192>(sx, xo[2], bx);
      sw += 32 * HD * 2; sx += 64;
      dma16<0, WXS>(sw, wo[0], bw); dma16<0, WXS + 1024>(sw, wo[1], bw); dma16<0, WXS + 2048>(sw, wo[2], bw);
      dma16<0, WXS>(sx, xo[0], bx); dma16<0, WXS + 4096>(sx, xo[1], bx); if (w == 0) dma16<0, WXS + 8192>(sx, xo[2], bx);
    };
    pair_issue(0); pair_issue(1);
    WAIT_BAR(10);
    for (int p = 0; p < 8; ++p) {
      pair_issue(p + 2);
      if (p >= 6) {
        const char* vp = c.vg + (size_t)(p - 5) * CHB; const unsigned d = dv + (p - 5) * CHB;
        DMAV4P(vp, d);
        WAIT_BAR(10);
      } else { WAIT_BAR(10); }
    }
    DMAK4(); ADVK(); DMAV4();
    DMAK4(); ADVK();
    vptr += 3 * CHB;
    if (dm) { WAIT_BAR(12); } else { WAIT_BAR(0); }
    DMAK4(); ADVK();
    WAIT_BAR(12);
    WAIT_BAR(63);
    readq();
    WAIT_BAR(63);
    newhead(std::false_type{}, 0.f, nullptr);
    WAIT_BAR(4);
  }

  for (int e = 0; e < 4; ++e) {
    const bool last = (e == 3);
    step(sB, sA, true, e > 0);
    if (e == 0) { WAIT_BAR(4); } else { WAIT_BAR(20); }
    for (int i = 1; i < 13; i += 2) {
      step(sA, sB, true, true); WAIT_BAR(8);
      step(sB, sA, true, true); WAIT_BAR(8);
    }
    step(sA, sB, !last, true);  if (last) { WAIT_BAR(4); } else { WAIT_BAR(8); }
    step(sB, sA, !last, !last); if (last) { WAIT_BAR(0); } else { WAIT_BAR(8); }
    {
      if (!last) { DMAK4(); ADVK(); DMAV4(); ADVV(); }
      SB();
      o[0] = MFMA32(f0, pf1, o[0]); f0 = VRD(0); sB[6] = EXP2M(sB[6]); sB[7] = EXP2M(sB[7]); PIN(sB); SB();
      o[1] = MFMA32(f1, pf1, o[1]); f1 = VRD(1); sB[8] = EXP2M(sB[8]); sB[9] = EXP2M(sB[9]); PIN(sB); SB();
      o[2] = MFMA32(f2, pf1, o[2]); f2 = VRD(2); pf0 = pack8(sB[0], sB[1], sB[2], sB[3], sB[4], sB[5], sB[6], sB[7]); PIN(pf0); SB();
      o[3] = MFMA32(f3, pf1, o[3]); f3 = VRD(3); sB[10] = EXP2M(sB[10]); sB[11] = EXP2M(sB[11]); PIN(sB); SB();
      o[4] = MFMA32(f4, pf1, o[4]); f4 = VRD(4); sB[12] = EXP2M(sB[12]); sB[13] = EXP2M(sB[13]); PIN(sB); SB();
      o[5] = MFMA32(f5, pf1, o[5]); f5 = VRD(5); sB[14] = EXP2M(sB[14]); sB[15] = EXP2M(sB[15]); PIN(sB); SB();
      rescale();
      SB();
      float sum;
      o[0] = MFMA32(f0, pf0, o[0]); f0 = VRD(6); pf1 = pack8(sB[8], sB[9], sB[10], sB[11], sB[12], sB[13], sB[14], sB[15]); PIN(pf1); SB();
      o[1] = MFMA32(f1, pf0, o[1]); f1 = VRD(7); sum = sB[0] + sB[1]; sum += sB[2]; sum += sB[3]; PIN(sum); SB();
      o[2] = MFMA32(f2, pf0, o[2]); f2 = VRD(8); sum += sB[4]; sum += sB[5]; sum += sB[6]; sum += sB[7]; PIN(sum); SB();
      o[3] = MFMA32(f3, pf0, o[3]); f3 = VRD(9); sum += sB[8]; sum += sB[9]; sum += sB[10]; sum += sB[11]; PIN(sum); SB();
      o[4] = MFMA32(f4, pf0, o[4]); f4 = VRD(10); sum += sB[12]; sum += sB[13]; sum += sB[14]; sum += sB[15]; l += sum; PIN(l); SB();
      o[5] = MFMA32(f5, pf0, o[5]); f5 = VRD(11); SB();
      float inv = 1.0f / xhalf_sum(l);
      o[0] = MFMA32(f0, pf1, o[0]); o[1] = MFMA32(f1, pf1, o[1]); o[2] = MFMA32(f2, pf1, o[2]);
      o[3] = MFMA32(f3, pf1, o[3]); o[4] = MFMA32(f4, pf1, o[4]); o[5] = MFMA32(f5, pf1, o[5]);
      PIN(inv);
      vs_rd = inc3(vs_rd);
      char* op = c.og + (size_t)(w * 384 + (c.h0 + e) * 12) * 1024;
      if (!last) {
        WAIT_BAR(63);
        readq();
        WAIT_BAR(63);
      }
      newhead(std::true_type{}, inv, op);
      if (last) break;
      WAIT_BAR(20);
    }
  }
#undef KRD
#undef VRD
#undef DMAK4
#undef DMAV4
#undef DMAV4P
#undef ADVK
#undef ADVV
#undef EXP2M
}

struct WxDma {
  unsigned woff[4], xoff[3];
  unsigned dw, dx;
  DI void init(unsigned lds0, int j, int lane) {
#pragma unroll
    for (int ii = 0; ii < 4; ++ii) {
      unsigned p = 64u * (4 * (j - 1) + ii) + lane, row = (p >> 3) / 3;
      unsigned cc = (p ^ swv(row)) - 24u * row;
      woff[ii] = row * (HD * 2) + 16u * cc;
    }
#pragma unroll
    for (int ii = 0; ii < 3; ++ii) {
      unsigned xi = 3u * (j - 1) + ii;
      unsigned r = 16u * xi + (lane >> 2);
      xoff[ii] = r * (DM * 2) + 16u * ((lane & 3) ^ ((r >> 2) & 3));
    }
    dw = lds0 + LDS_WX + 4096u * (j - 1); dx = lds0 + LDS_WX + CHB + 3072u * (j - 1);
  }
  template <int II> DI void w(const Ctx& c, int e, int ch, int slot) const {
    dma16<0, 1024 * II>(c.wq + (size_t)ch * (32 * HD * 2) + e * (DH * 2), woff[II], dw + slot * WXS);
  }
  template <int II> DI void x(const Ctx& c, int ch, int slot) const { dma16<0, 1024 * II>(c.xg + ch * 64, xoff[II], dx + slot * WXS); }
};

DI void proj_wave(const Ctx& c, lds_char* lds, int j, int lane) {
  const unsigned lds0 = (unsigned)(uintptr_t)lds;
  const int r32 = lane & 31, h2 = lane >> 5;
  WxDma dma; dma.init(lds0, j, lane);
  unsigned wb[2][2];
  {
    const unsigned blk = (lane >> 4) & 1, q4 = (lane & 15) >> 2, p = lane & 3;
#pragma unroll
    for (int tt = 0; tt < 2; ++tt)
#pragma unroll
      for (int a = 0; a < 2; ++a)
        wb[tt][a] = LDS_WX + 384u * (8u * h2 + 4u * a + q4) + 128u * (j - 1) +
                    16u * (4u * (tt ^ (q4 >> 1)) + 2u * (blk ^ a) + (p & 1)) + 8u * (p >> 1);
  }
  unsigned xb[2];
#pragma unroll
  for (int sp = 0; sp < 2; ++sp) xb[sp] = LDS_WX + CHB + 64u * r32 + 16u * ((2u * sp + h2) ^ ((r32 >> 2) & 3));
  f32x16 acc[4][2];
  bf16x8 aA[2], bA[4], aB[2], bB[4];
#define KSTEP(ar, br, slot, sp, am, bm, D0, D1, D2, D3, D4, D5, D6) do { \
    const lds_char* bs_ = lds + (slot) * WXS; \
    acc[0][0] = MFMA32(am[0], bm[0], acc[0][0]); ar[0] = tr2(bs_ + wb[0][0] + 6144 * (sp), bs_ + wb[0][1] + 6144 * (sp)); SB(); \
    acc[0][1] = MFMA32(am[1], bm[0], acc[0][1]); br[0] = ldsr(bs_ + xb[sp]); D0; SB(); \
    acc[1][0] = MFMA32(am[0], bm[1], acc[1][0]); ar[1] = tr2(bs_ + wb[1][0] + 6144 * (sp), bs_ + wb[1][1] + 6144 * (sp)); D1; SB(); \
    acc[1][1] = MFMA32(am[1], bm[1], acc[1][1]); br[1] = ldsr(bs_ + xb[sp] + 2048); D2; SB(); \
    acc[2][0] = MFMA32(am[0], bm[2], acc[2][0]); br[2] = ldsr(bs_ + xb[sp] + 4096); D3; SB(); \
    acc[2][1] = MFMA32(am[1], bm[2], acc[2][1]); br[3] = ldsr(bs_ + xb[sp] + 6144); D4; SB(); \
    acc[3][0] = MFMA32(am[0], bm[3], acc[3][0]); D5; SB(); \
    acc[3][1] = MFMA32(am[1], bm[3], acc[3][1]); D6; SB(); } while (0)
#define DW(e_, ch_, sl_, ii) dma.template w<ii>(c, e_, ch_, sl_)
#define DX(ch_, sl_, ii) dma.template x<ii>(c, ch_, sl_)
#define NOP (void)0
#define CH6(e_, ch_, sl_) DW(e_, ch_, sl_, 0), DW(e_, ch_, sl_, 1), DW(e_, ch_, sl_, 2), DW(e_, ch_, sl_, 3), DX(ch_, sl_, 0), DX(ch_, sl_, 1), DX(ch_, sl_, 2)
#define CH6C(cond, e_, ch_, sl_) if (cond) DW(e_, ch_, sl_, 0), if (cond) DW(e_, ch_, sl_, 1), if (cond) DW(e_, ch_, sl_, 2), if (cond) DW(e_, ch_, sl_, 3), if (cond) DX(ch_, sl_, 0), if (cond) DX(ch_, sl_, 1), if (cond) DX(ch_, sl_, 2)
#define KSTEP_(...) KSTEP(__VA_ARGS__)
#define KS(ar, br, slot, sp, am, bm) KSTEP(ar, br, slot, sp, am, bm, NOP, NOP, NOP, NOP, NOP, NOP, NOP)
#define CH7(e_, ch_, sl_) do { DW(e_, ch_, sl_, 0); DW(e_, ch_, sl_, 1); DW(e_, ch_, sl_, 2); DW(e_, ch_, sl_, 3); DX(ch_, sl_, 0); DX(ch_, sl_, 1); DX(ch_, sl_, 2); } while (0)

  auto zero = [&]() {
#pragma unroll
    for (int w = 0; w < 4; ++w) { acc[w][0] = f32x16{}; acc[w][1] = f32x16{}; bB[w] = bf16x8{}; }
    aB[0] = bf16x8{}; aB[1] = bf16x8{};
  };
  auto handoff = [&]() {
#pragma unroll
    for (int w = 0; w < 4; ++w) { acc[w][0] = MFMA32(aB[0], bB[w], acc[w][0]); acc[w][1] = MFMA32(aB[1], bB[w], acc[w][1]); }
#pragma unroll
    for (int w = 0; w < 4; ++w)
#pragma unroll
      for (int tt = 0; tt < 2; ++tt)
#pragma unroll
        for (int sp = 0; sp < 2; ++sp) {
          const f32x16& a = acc[w][tt];
          bf16x8 f = pack8(a[8 * sp], a[8 * sp + 1], a[8 * sp + 2], a[8 * sp + 3], a[8 * sp + 4], a[8 * sp + 5], a[8 * sp + 6], a[8 * sp + 7]);
          *(lds_bf16x8*)(lds + LDS_Q + (w * 12 + 4 * (j - 1) + 2 * tt + sp) * 1024 + lane * 16) = f;
        }
  };
  zero();
  WAIT_BAR(63);
  for (int p = 0; p < 10; ++p) {
    const int pr = (p + 1) % 3;
    lds_char* const lsave = lds;
    lds = lsave + (pr == 2 ? -LDS_WX : 2 * pr * WXS);
    KS(aA, bA, 0, 0, aB, bB);
    KS(aB, bB, 0, 1, aA, bA);
    KS(aA, bA, 1, 0, aB, bB);
    KS(aB, bB, 1, 1, aA, bA);
    lds = lsave;
    if (p == 9) CH7(1, 0, 0);
    WAIT_BAR(63);
  }
  for (int e = 1; e <= 4; ++e) {
    handoff();
    WAIT_BAR(63);
    WAIT_BAR(63);
    if (e == 4) { WAIT_BAR(63); break; }
    CH7(e, 1, 1); CH7(e, 2, 2); CH7(e, 3, 3);
    zero();
    WAIT_BAR(21);
    for (int mm = 0; mm < 5; ++mm) {
      const int c4 = 4 * mm;
      const bool lastm = (mm == 4);
      if (WXPOS == 0 && mm > 0) { CH7(e, c4 + 2, 2); CH7(e, c4 + 3, 3); }
      KS(aA, bA, 0, 0, aB, bB);
      KS(aB, bB, 0, 1, aA, bA);
      if (WXPOS == 1 && mm > 0) { CH7(e, c4 + 2, 2); CH7(e, c4 + 3, 3); }
      WAIT_BAR(14);
      if (WXPOS == 0 && !lastm) CH7(e, c4 + 4, 0);
      KS(aA, bA, 1, 0, aB, bB);
      KS(aB, bB, 1, 1, aA, bA);
      if (WXPOS == 1 && !lastm) CH7(e, c4 + 4, 0);
      if (lastm) { WAIT_BAR(0); } else { WAIT_BAR(7); }
      {
        const int e2 = lastm ? e + 1 : e, ch2 = lastm ? 0 : c4 + 5, sl2 = lastm ? 0 : 1;
        if (WXPOS == 0 && e2 < 4) CH7(e2, ch2, sl2);
        KS(aA, bA, 2, 0, aB, bB);
        KS(aB, bB, 2, 1, aA, bA);
        KS(aA, bA, 3, 0, aB, bB);
        KS(aB, bB, 3, 1, aA, bA);
        if (WXPOS == 1 && e2 < 4) CH7(e2, ch2, sl2);
        WAIT_BAR(7);
      }
    }
  }
  for (int i = 0; i < 15; ++i) { WAIT_BAR(63); }
#undef KSTEP
#undef KSTEP_
#undef CH6
#undef CH6C
#undef DX
#undef DW
#undef NOP
}

DI void q8_wave(const Ctx& c, lds_char* lds, int lane) {
  const unsigned lds0 = (unsigned)(uintptr_t)lds;
  const unsigned r16 = lane & 15, g = lane >> 4, q4 = (lane & 15) >> 2, p = lane & 3;
  unsigned wb[2][2];
#pragma unroll
  for (int s1 = 0; s1 < 2; ++s1)
#pragma unroll
    for (int a = 0; a < 2; ++a)
      wb[s1][a] = LDS_WX + 384u * (8u * g + 4u * a + q4) + 16u * (4u * (s1 ^ (q4 >> 1)) + (p ^ (a << 1)));
  const unsigned xb = LDS_WX + CHB + 64u * (128u + r16) + 16u * (g ^ ((r16 >> 2) & 3));
  unsigned kb[2];
#pragma unroll
  for (int s1 = 0; s1 < 2; ++s1) kb[s1] = LDS_K + 384u * r16 + 16u * ((4u * s1 + g) ^ ((r16 >> 1) & 7));
  unsigned vb[4];
#pragma unroll
  for (int T = 0; T < 4; ++T)
    vb[T] = LDS_V + 384u * (4u * g + q4) + 16u * (4u * ((T >> 1) ^ (q4 >> 1)) + 2u * ((T & 1) ^ (g & 1)) + (p >> 1)) + 8u * (p & 1);

  constexpr int TR = 32 / NBX, TL = 3 * TR;
  constexpr int TCH = (64 / TL) < 4 ? (64 / TL) : 4;
  unsigned toff;
  { const unsigned ll = lane < TL * TCH ? lane : TL * TCH - 1; toff = ((ll / TL) * 32u + TR * c.b + (ll % TL) / 3) * (HD * 2) + ((ll % TL) % 3) * 128u; }
  auto touchw = [&](int e, int ch) {
#pragma unroll
    for (int q = 0; q < 4 / TCH; ++q) touch4(c.wq + (size_t)(ch + q * TCH) * (32 * HD * 2) + e * (DH * 2), toff, lds0 + LDS_SCR);
  };
  constexpr int XS = 32 / NBX;
  constexpr int XR = SQ / XS;
  constexpr int XI = (2 * XR + 63) / 64;
  unsigned xtoff[XI];
#pragma unroll
  for (int q = 0; q < XI; ++q) { unsigned ll = q * 64 + lane; if (ll >= 2 * XR) ll = 2 * XR - 1; xtoff[q] = (XR * c.xs + (ll >> 1)) * (DM * 2) + (ll & 1) * 128u; }
  auto touchx0 = [&](int ch) {
#pragma unroll
    for (int q = 0; q < XI; ++q) touch4(c.xg + ch * 64, xtoff[q], lds0 + LDS_SCR);
  };
  auto touchx = [&](int ch) {
  };
#pragma unroll
  for (int g4 = 0; g4 < 5; ++g4) { touchw(0, 4 * g4); touchx0(4 * g4); }
  f32x4 qa[12], o[12];
  bf16x8 qf[6];
#pragma unroll
  for (int T = 0; T < 12; ++T) { o[T] = f32x4{}; qa[T] = f32x4{}; }
  float m = 0.f, mc = 0.f, l = 0.f, alpha = 1.f;
  bool resc = false;
  bf16x8 pf = {};
  bf16x8 fb[8] = {};
  f32x4 p0 = {}, p1 = {};
  int ks_rd = 0, vs_rd = 0;

#define KRD(i) ldsr(lds + ks_rd + kb[((i) >> 1) & 1] + 128 * ((i) >> 2) + 6144 * ((i) & 1))
#define VRD(T) tr2(lds + vs_rd + vb[(T) & 3] + 128 * ((T) >> 2), lds + vs_rd + vb[(T) & 3] + 128 * ((T) >> 2) + 6144)
#define WRD(slot, T) tr2(lds + (slot) * WXS + wb[((T) >> 1) & 1][0] + 128 * ((T) >> 2) + 8 * ((T) & 1), lds + (slot) * WXS + wb[((T) >> 1) & 1][1] + 128 * ((T) >> 2) + 8 * ((T) & 1))
#define EXP2M(x) __builtin_amdgcn_exp2f(__builtin_fmaf((x), QSCALE, mc))

  auto rescale = [&]() {
    asm volatile("" ::: "memory");
    if (__builtin_expect(resc, 0)) {
#pragma unroll
      for (int T = 0; T < 12; ++T)
#pragma unroll
        for (int r = 0; r < 4; ++r) o[T][r] *= alpha;
      resc = false;
    }
  };
  auto decide = [&](float mx) {
    asm volatile("" ::: "memory");
    if (__builtin_expect(__any(mx - m > THR / QSCALE), 0)) {
      float mn = __builtin_fmaxf(m, mx);
      alpha = __builtin_amdgcn_exp2f((m - mn) * QSCALE);
      m = mn; mc = -mn * QSCALE; l *= alpha; resc = true;
    }
  };
  auto chunk = [&](int slot) {
    bf16x8 b = ldsr(lds + slot * WXS + xb);
    bf16x8 a0 = WRD(slot, 0), a1 = WRD(slot, 1), a2 = WRD(slot, 2), a3 = WRD(slot, 3);
    SB();
    qa[0] = MFMA16(a0, b, qa[0]); a0 = WRD(slot, 4); SB();
    qa[1] = MFMA16(a1, b, qa[1]); a1 = WRD(slot, 5); SB();
    qa[2] = MFMA16(a2, b, qa[2]); a2 = WRD(slot, 6); SB();
    qa[3] = MFMA16(a3, b, qa[3]); a3 = WRD(slot, 7); SB();
    qa[4] = MFMA16(a0, b, qa[4]); a0 = WRD(slot, 8); SB();
    qa[5] = MFMA16(a1, b, qa[5]); a1 = WRD(slot, 9); SB();
    qa[6] = MFMA16(a2, b, qa[6]); a2 = WRD(slot, 10); SB();
    qa[7] = MFMA16(a3, b, qa[7]); a3 = WRD(slot, 11); SB();
    qa[8] = MFMA16(a0, b, qa[8]); qa[9] = MFMA16(a1, b, qa[9]); qa[10] = MFMA16(a2, b, qa[10]); qa[11] = MFMA16(a3, b, qa[11]); SB();
  };
  auto stepq = [&](auto nch_, auto sl_) {
    constexpr int NCH = decltype(nch_)::value, SL = decltype(sl_)::value;
    constexpr int NOPS = 24 + 12 * NCH, PV0 = 20 + 12 * NCH;
    f32x4 s0, s1; bf16x8 bx0, bx1; float mx;
    static_for<NOPS>([&](auto k_) {
      constexpr int k = decltype(k_)::value, b = k & 7;
      if constexpr (k < 8) { o[4 + k] = MFMA16(fb[b], pf, o[4 + k]); }
      else if constexpr (k < 20) {
        constexpr int i = k - 8;
        if constexpr (i == 0) s0 = MFMA16(fb[b], qf[0], (f32x4{}));
        else if constexpr (i == 1) s1 = MFMA16(fb[b], qf[0], (f32x4{}));
        else if constexpr (i & 1) s1 = MFMA16(fb[b], qf[i >> 1], s1);
        else s0 = MFMA16(fb[b], qf[i >> 1], s0);
      } else if constexpr (k < PV0) {
        constexpr int T = (k - 20) % 12, cc = (k - 20) / 12;
        qa[T] = MFMA16(fb[b], (cc ? bx1 : bx0), qa[T]);
      } else { o[k - PV0] = MFMA16(fb[b], pf, o[k - PV0]); }
      constexpr int n = k + 8;
      if constexpr (n < 20) fb[b] = KRD(n - 8);
      else if constexpr (n < PV0) fb[b] = WRD(SL + (n - 20) / 12, (n - 20) % 12);
      else if constexpr (n < NOPS) fb[b] = VRD(n - PV0);
      else fb[b] = VRD(4 + b);
      if constexpr (k == 2 && NCH >= 1) bx0 = ldsr(lds + SL * WXS + xb);
      if constexpr (k == 4 && NCH >= 2) bx1 = ldsr(lds + (SL + 1) * WXS + xb);
      if constexpr (k == 5) { float sum = (p0[0] + p0[1]) + (p0[2] + p0[3]) + (p1[0] + p1[1]) + (p1[2] + p1[3]); l += sum; PIN(l); }
      if constexpr (k == 9) { pf = pack8(p0[0], p0[1], p0[2], p0[3], p1[0], p1[1], p1[2], p1[3]); PIN(pf); }
      if constexpr (k == 12) { SB(); rescale(); }
      if constexpr (k == 21) {
        mx = max3(s0[0], s0[1], s0[2]);
        mx = max3(mx, s0[3], s1[0]); mx = max3(mx, s1[1], s1[2]); mx = __builtin_fmaxf(mx, s1[3]);
        mx = x16_max(xhalf_max(mx));
        SB(); decide(mx);
      }
      if constexpr (k == 22) { p0[0] = EXP2M(s0[0]); p0[1] = EXP2M(s0[1]); p0[2] = EXP2M(s0[2]); p0[3] = EXP2M(s0[3]); PIN(p0); }
      if constexpr (k == 23) { p1[0] = EXP2M(s1[0]); p1[1] = EXP2M(s1[1]); p1[2] = EXP2M(s1[2]); p1[3] = EXP2M(s1[3]); PIN(p1); }
      SB();
    });
    ks_rd = inc3(ks_rd); vs_rd = inc3(vs_rd);
  };
  auto newhead = [&]() {
#pragma unroll
    for (int s = 0; s < 6; ++s)
      qf[s] = pack8(qa[2 * s][0], qa[2 * s][1], qa[2 * s][2], qa[2 * s][3], qa[2 * s + 1][0], qa[2 * s + 1][1], qa[2 * s + 1][2], qa[2 * s + 1][3]);
#pragma unroll
    for (int T = 0; T < 12; ++T) qa[T] = f32x4{};
    f32x4 s0 = {}, s1 = {};
#pragma unroll
    for (int s = 0; s < 6; ++s) { bf16x8 a0 = KRD(2 * s), a1 = KRD(2 * s + 1); s0 = MFMA16(a0, qf[s], s0); s1 = MFMA16(a1, qf[s], s1); }
    float mx = max3(s0[0], s0[1], s0[2]);
    mx = max3(mx, s0[3], s1[0]); mx = max3(mx, s1[1], s1[2]); mx = __builtin_fmaxf(mx, s1[3]);
    m = x16_max(xhalf_max(mx)); mc = -m * QSCALE; l = 0.f; resc = false;
#pragma unroll
    for (int r = 0; r < 4; ++r) { p0[r] = EXP2M(s0[r]); p1[r] = EXP2M(s1[r]); }
    PIN(p0); PIN(p1);
    ks_rd = inc3(ks_rd);
  };

  using I0 = std::integral_constant<int, 0>; using I1 = std::integral_constant<int, 1>; using I2 = std::integral_constant<int, 2>;
  auto finish = [&](int e) {
#pragma unroll
    for (int k = 0; k < 8; ++k) o[4 + k] = MFMA16(fb[k], pf, o[4 + k]);
    rescale();
    l += (p0[0] + p0[1]) + (p0[2] + p0[3]) + (p1[0] + p1[1]) + (p1[2] + p1[3]);
    pf = pack8(p0[0], p0[1], p0[2], p0[3], p1[0], p1[1], p1[2], p1[3]);
#pragma unroll
    for (int T = 0; T < 12; ++T) { bf16x8 a = VRD(T); o[T] = MFMA16(a, pf, o[T]); }
    vs_rd = inc3(vs_rd);
    const float inv = 1.0f / x16_sum(xhalf_sum(l));
    char* op = c.og + (size_t)4 * 384 * 1024 + ((size_t)((c.h0 + e) * 6) * 64 + lane) * 16;
#pragma unroll
    for (int s = 0; s < 6; ++s) {
      bf16x8 f = pack8(o[2 * s][0] * inv, o[2 * s][1] * inv, o[2 * s][2] * inv, o[2 * s][3] * inv,
                       o[2 * s + 1][0] * inv, o[2 * s + 1][1] * inv, o[2 * s + 1][2] * inv, o[2 * s + 1][3] * inv);
      OSTORE(f, op + s * 1024);
      o[2 * s] = f32x4{}; o[2 * s + 1] = f32x4{};
    }
#pragma unroll
    for (int k = 0; k < 8; ++k) fb[k] = bf16x8{};
  };
  WAIT_BAR(63);
  for (int p = 0; p < 10; ++p) {
    const int pr = (p + 1) % 3;
    if (p == 4) touchw(1, 0);
    if (p == 7) touchw(1, 4);
    lds_char* const lsave = lds;
    lds = lsave + (pr == 2 ? -LDS_WX : 2 * pr * WXS);
    chunk(0); chunk(1);
    lds = lsave;
    WAIT_BAR(63);
  }
  WAIT_BAR(63);
  WAIT_BAR(63);
  newhead();
  WAIT_BAR(63);
  for (int e = 0; e < 3; ++e) {
    for (int mm = 0; mm < 5; ++mm) {
      { const int g4 = mm + TOUCH_AHEAD; if (g4 < 5) { touchw(e + 1, 4 * g4); touchx(4 * g4); } else if (e < 2) { touchw(e + 2, 4 * (g4 - 5)); touchx(4 * (g4 - 5)); } }
      stepq(I1{}, I0{}); WAIT_BAR(63);
      stepq(I1{}, I1{}); WAIT_BAR(63);
      stepq(I2{}, I2{}); WAIT_BAR(63);
    }
    finish(e);
    WAIT_BAR(63);
    WAIT_BAR(63);
    newhead();
    WAIT_BAR(63);
  }
  for (int i = 0; i < 15; ++i) { stepq(I0{}, I0{}); WAIT_BAR(63); }
  finish(3);
#undef KRD
#undef VRD
#undef WRD
#undef EXP2M
}

__global__ __launch_bounds__(512, 2) void attn_fused(const bf16* xA, const bf16* kA, const bf16* vA, const bf16* wqA,
                                                     const bf16* xB, const bf16* kB, const bf16* vB, const bf16* wqB, char* ows) {
  extern __shared__ __attribute__((aligned(16))) char smem[];
  lds_char* lds = (lds_char*)smem;
  const int g = blockIdx.x, xcd = g & 7, jj = g >> 3;
  const int br = xcd >> 2, bl = jj & 7, b = 8 * ((xcd >> 1) & 1) + bl, h0 = 4 * (4 * (xcd & 1) + (jj >> 3));
  const int wave = __builtin_amdgcn_readfirstlane(threadIdx.x >> 6), lane = threadIdx.x & 63;
  Ctx c;
  const size_t kvo = (size_t)(b * NH + h0) * SK * DH * 2;
  c.kg = (const char*)(br ? kB : kA) + kvo;
  c.vg = (const char*)(br ? vB : vA) + kvo;
  c.wq = (const char*)(br ? wqB : wqA) + h0 * DH * 2;
  c.xg = (const char*)(br ? xB : xA) + (size_t)b * SQ * DM * 2;
  c.og = ows + (size_t)(br * NB + b) * O_PER_B;
  c.h0 = h0; c.b = bl; c.xs = jj / NBX;
  if (threadIdx.x == 0) *(unsigned*)(ows + (size_t)2 * NB * O_PER_B + g * 128) = 0u;
  if (wave < 4) main_wave(c, lds, wave, lane);
  else if (wave == 4) {
    __builtin_amdgcn_s_setprio(2);
    q8_wave(c, lds, lane); }
  else proj_wave(c, lds, wave - 4, lane);
}

constexpr int P_WOB = 32 * 320;
constexpr int P_OB = 18 * 1024;
constexpr int P_CH = P_WOB + P_OB;
constexpr int P_NSLOT = 5, P_LDS = P_NSLOT * P_CH;
constexpr int P_NSTEP = 48, P_LA = 4;
#define P_PRIO 0
#define P_LEAD 8
constexpr size_t WS_O_BYTES = (size_t)2 * NB * O_PER_B;
constexpr size_t WS_FLAG = WS_O_BYTES;
constexpr size_t WS_XCH = WS_FLAG + 256 * 128;
constexpr int XCH_W = 20480 + 3072;
constexpr int XCH_BYTES = 4 * XCH_W;

DI unsigned long long stamp2() { unsigned long long t; asm volatile("s_memtime %0\n\ts_waitcnt lgkmcnt(0)" : "=s"(t) :: "memory"); return t; }
DI void st_sc1(char* sb, unsigned vo, f32x4 v) { asm volatile("global_store_dwordx4 %0, %1, %2 sc1\n\ts_nop 1" :: "v"(vo), "v"(v), "s"(sb) : "memory"); }
DI f32x4 ld_sc1(const char* sb, unsigned vo) { f32x4 v; asm volatile("global_load_dwordx4 %0, %1, %2 sc1" : "=v"(v) : "v"(vo), "s"(sb) : "memory"); return v; }

__global__ __launch_bounds__(512, 2) void oproj(const char* ows, const bf16* woA, const bf16* woB, bf16* out, char* wsx) {
  extern __shared__ __attribute__((aligned(16))) char smem[];
  lds_char* lds = (lds_char*)smem;
  const unsigned lds0 = (unsigned)(uintptr_t)lds;
  const int g = blockIdx.x, xcd = g & 7, jj = g >> 3;
  const int br = xcd >> 2, kq = xcd & 3, bp = jj & 7, nq = jj >> 3;
  const int wave = __builtin_amdgcn_readfirstlane(threadIdx.x >> 6), lane = threadIdx.x & 63;
  const char* og = ows + (size_t)(br * NB + 2 * bp) * O_PER_B;
  const char* wo = (const char*)(br ? woB : woA) + (size_t)kq * 1536 * (DM * 2) + nq * 320;
  const int g0 = g & ~3;
  char* xbase = wsx + (WS_XCH - WS_FLAG);
  bf16* orow0 = out + (size_t)(2 * bp * SQ) * (2 * DM) + br * DM + nq * 160;
  const unsigned l16 = lane * 16;
#define P_BAR(N) do { SB(); asm volatile("s_waitcnt vmcnt(" #N ") lgkmcnt(0)\n\ts_barrier" ::: "memory"); SB(); } while (0)

#define P_BARM(N) do { SB(); asm volatile("s_waitcnt vmcnt(" #N ") lgkmcnt(0)\n\ts_barrier" ::: "memory"); SB(); } while (0)
  if (wave < 4) {
    const int w = wave, h2 = lane >> 5;
    const unsigned blk = (lane >> 4) & 1, q4 = (lane & 15) >> 2, p = lane & 3, gq = lane >> 4;
    const unsigned ab = 320u * (4u * h2 + q4) + 8u * (4u * (p & 1) + 2u * blk + (p >> 1));
    const unsigned bb = P_WOB + 4096u * w + l16;
    const int T0 = w < 2 ? 3 * w : 2 * w + 2;
    const unsigned ab8 = 320u * (4u * gq + q4) + 8u * p + 32u * T0;
    const unsigned bb8 = P_WOB + 16384u + l16;
    f32x16 acc[2][5];
#pragma unroll
    for (int t = 0; t < 5; ++t) { acc[0][t] = f32x16{}; acc[1][t] = f32x16{}; }
    f32x4 ac8[2][3] = {};
    bf16x8 a[5] = {}, bA[2] = {}, bB[2] = {};
    bf16x8 a8 = {}, b8[2] = {};
#define P_KS(br_, off, s, bm, X0, X1, X2, X3, X4) do { \
      const lds_char* pa_ = lds + (off) + ab + 5120 * (s); \
      acc[0][0] = MFMA32(a[0], bm[0], acc[0][0]); br_[0] = ldsr(lds + (off) + bb + 1024 * (s)); SB(); \
      acc[1][0] = MFMA32(a[0], bm[1], acc[1][0]); a[0] = tr2(pa_, pa_ + 2560); SB(); \
      acc[0][1] = MFMA32(a[1], bm[0], acc[0][1]); br_[1] = ldsr(lds + (off) + bb + 2048 + 1024 * (s)); X0; SB(); \
      acc[1][1] = MFMA32(a[1], bm[1], acc[1][1]); a[1] = tr2(pa_ + 64, pa_ + 2560 + 64); SB(); \
      acc[0][2] = MFMA32(a[2], bm[0], acc[0][2]); X1; SB(); \
      acc[1][2] = MFMA32(a[2], bm[1], acc[1][2]); a[2] = tr2(pa_ + 128, pa_ + 2560 + 128); SB(); \
      acc[0][3] = MFMA32(a[3], bm[0], acc[0][3]); X2; SB(); \
      acc[1][3] = MFMA32(a[3], bm[1], acc[1][3]); a[3] = tr2(pa_ + 192, pa_ + 2560 + 192); SB(); \
      acc[0][4] = MFMA32(a[4], bm[0], acc[0][4]); X3; SB(); \
      acc[1][4] = MFMA32(a[4], bm[1], acc[1][4]); a[4] = tr2(pa_ + 256, pa_ + 2560 + 256); X4; SB(); } while (0)
#define M8(t) (ac8[0][t] = MFMA16(a8, b8[0], ac8[0][t]), ac8[1][t] = MFMA16(a8, b8[1], ac8[1][t]))
#define R8(off, t) a8 = tr2(lds + (off) + ab8 + 32 * (t), lds + (off) + ab8 + 32 * (t) + 5120)
#define NOP (void)0
    P_BARM(63);
    int so = 0;
    for (int st = 0; st < P_NSTEP; ++st) {
      P_KS(bA, so, 0, bB, b8[0] = ldsr(lds + so + bb8), (b8[1] = ldsr(lds + so + bb8 + 1024), R8(so, 0)), NOP, (M8(0), R8(so, 1)), NOP);
      P_KS(bB, so, 1, bA, NOP, (M8(1), R8(so, 2)), NOP, M8(2), NOP);
      so = (so == (P_NSLOT - 1) * P_CH) ? 0 : so + P_CH;
      P_BARM(63);
    }
#pragma unroll
    for (int t = 0; t < 5; ++t) { acc[0][t] = MFMA32(a[t], bB[0], acc[0][t]); acc[1][t] = MFMA32(a[t], bB[1], acc[1][t]); }
#undef P_KS
#undef M8
#undef R8
#undef NOP
    unsigned ln;
    asm volatile("v_mbcnt_lo_u32_b32 %0, -1, 0\n\tv_mbcnt_hi_u32_b32 %0, -1, %0" : "=v"(ln));
#define l16 (ln * 16u)
#define lane ((int)ln)
#define h2 ((int)(ln >> 5))
#define gq (ln >> 4)
    {
      char* xs = xbase + (size_t)g0 * XCH_BYTES + kq * XCH_W + w * 5120;
      lds_char* own = lds + w * 10240 + l16;
#pragma unroll
      for (int pt = 0; pt < 4; ++pt) {
        if (pt == kq) {
#pragma unroll
          for (int i = 0; i < 10; ++i) {
            const int gi = 10 * pt + i; const f32x16& a = acc[gi / 20][(gi % 20) >> 2];
            f32x4 v = {a[4 * (gi & 3)], a[4 * (gi & 3) + 1], a[4 * (gi & 3) + 2], a[4 * (gi & 3) + 3]};
            *(__attribute__((address_space(3))) f32x4*)(own + i * 1024) = v;
          }
        } else {
#pragma unroll
          for (int i = 0; i < 5; ++i) {
            const int gi = 10 * pt + 2 * i; const f32x16& a = acc[gi / 20][(gi % 20) >> 2]; const int r0 = 4 * (gi & 3);
            bf16x8 f = pack8(a[r0], a[r0 + 1], a[r0 + 2], a[r0 + 3], a[r0 + 4], a[r0 + 5], a[r0 + 6], a[r0 + 7]);
            st_sc1(xs + (size_t)pt * XCH_BYTES + i * 1024, l16, __builtin_bit_cast(f32x4, f));
          }
        }
      }
      if (w != kq) {
        char* x8 = xbase + (size_t)(g0 + w) * XCH_BYTES + kq * XCH_W + 20480;
#pragma unroll
        for (int t = 0; t < 3; ++t) {
          bf16x8 f = pack8(ac8[0][t][0], ac8[0][t][1], ac8[0][t][2], ac8[0][t][3], ac8[1][t][0], ac8[1][t][1], ac8[1][t][2], ac8[1][t][3]);
          st_sc1(x8 + t * 1024, l16, __builtin_bit_cast(f32x4, f));
        }
      }
    }
    P_BARM(0);
    if (w == 0) {
      if (lane == 0) __hip_atomic_store((unsigned*)(wsx + g * 128), 1u, __ATOMIC_RELAXED, __HIP_MEMORY_SCOPE_AGENT);
      unsigned* pf = (unsigned*)(wsx + (g0 + (lane & 3)) * 128);
      while (__any(__hip_atomic_load(pf, __ATOMIC_RELAXED, __HIP_MEMORY_SCOPE_AGENT) == 0u)) __builtin_amdgcn_s_sleep(1);
    }
    P_BARM(0);
    {
      const char* xr = xbase + (size_t)g * XCH_BYTES;
      f32x4 v[15], v8[9];
#pragma unroll
      for (int i = 0; i < 15; ++i) v[i] = ld_sc1(xr + ((kq + 1 + i / 5) & 3) * XCH_W + w * 5120 + (i % 5) * 1024, l16);
      if (w == kq) {
#pragma unroll
        for (int i = 0; i < 9; ++i) v8[i] = ld_sc1(xr + ((kq + 1 + i / 3) & 3) * XCH_W + 20480 + (i % 3) * 1024, l16);
      } else {
#pragma unroll
        for (int i = 0; i < 9; ++i) v8[i] = f32x4{};
      }
      asm volatile("s_waitcnt vmcnt(0)" : "+v"(v[0]), "+v"(v[1]), "+v"(v[2]), "+v"(v[3]), "+v"(v[4]), "+v"(v[5]), "+v"(v[6]), "+v"(v[7]), "+v"(v[8]), "+v"(v[9]),
                   "+v"(v[10]), "+v"(v[11]), "+v"(v[12]), "+v"(v[13]), "+v"(v[14]) :: "memory");
      asm volatile("" : "+v"(v8[0]), "+v"(v8[1]), "+v"(v8[2]), "+v"(v8[3]), "+v"(v8[4]), "+v"(v8[5]), "+v"(v8[6]), "+v"(v8[7]), "+v"(v8[8]) :: "memory");
      auto lo = [](float x) { return __uint_as_float(__float_as_uint(x) << 16); };
      auto hi = [](float x) { return __uint_as_float(__float_as_uint(x) & 0xffff0000u); };
      const lds_char* own = lds + w * 10240 + l16;
      bf16* orow = orow0 + (size_t)((w >> 1) * SQ + 64 * (w & 1) + 32 * (kq >> 1) + (lane & 31)) * (2 * DM) + 16 * h2;
#pragma unroll
      for (int pr = 0; pr < 5; ++pr) {
        f32x4 a = *(const __attribute__((address_space(3))) f32x4*)(own + (2 * pr) * 1024);
        f32x4 c2 = *(const __attribute__((address_space(3))) f32x4*)(own + (2 * pr + 1) * 1024);
#pragma unroll
        for (int sidx = 0; sidx < 3; ++sidx) {
          const f32x4 x = v[5 * sidx + pr];
          a[0] += lo(x[0]); a[1] += hi(x[0]); a[2] += lo(x[1]); a[3] += hi(x[1]);
          c2[0] += lo(x[2]); c2[1] += hi(x[2]); c2[2] += lo(x[3]); c2[3] += hi(x[3]);
        }
        const int gi = 10 * (kq & 1) + 2 * pr;
        bf16x8 f = pack8(a[0], a[1], a[2], a[3], c2[0], c2[1], c2[2], c2[3]);
        *(bf16x8*)(orow + 32 * (gi >> 2) + 4 * (gi & 3)) = f;
      }
      if (w == kq) {
#pragma unroll
        for (int t = 0; t < 3; ++t) {
          if (t < 2 || w < 2) {
            f32x4 a = ac8[0][t], c2 = ac8[1][t];
#pragma unroll
            for (int sidx = 0; sidx < 3; ++sidx) {
              const f32x4 x = v8[3 * sidx + t];
              a[0] += lo(x[0]); a[1] += hi(x[0]); a[2] += lo(x[1]); a[3] += hi(x[1]);
              c2[0] += lo(x[2]); c2[1] += hi(x[2]); c2[2] += lo(x[3]); c2[3] += hi(x[3]);
            }
            bf16* orow8 = orow0 + (size_t)(128 + (lane & 15)) * (2 * DM) + 16 * (T0 + t) + 4 * gq;
            uint2 f0 = {cvtpk(a[0], a[1]), cvtpk(a[2], a[3])}, f1 = {cvtpk(c2[0], c2[1]), cvtpk(c2[2], c2[3])};
            *(uint2*)orow8 = f0; *(uint2*)(orow8 + (size_t)SQ * (2 * DM)) = f1;
          }
        }
      }
    }
#undef l16
#undef lane
#undef h2
#undef gq
  } else {
    __builtin_amdgcn_s_setprio(P_PRIO);
    const int j = wave - 4;
    unsigned wofs[3];
#pragma unroll
    for (int ii = 0; ii < 3; ++ii) { unsigned pp = 64u * (j + 4 * ii) + lane; unsigned row = pp / 20; wofs[ii] = row * (DM * 2) + (pp - 20 * row) * 16; }
    const char* op = og + (size_t)(j >> 1) * O_PER_B + (size_t)(2 * (j & 1) * 384 + kq * 96) * 1024;
    const char* o8 = og + (size_t)(j & 1) * O_PER_B + (size_t)4 * 384 * 1024 + (size_t)(kq * 48) * 1024;
    const int rot = (2 * nq + bp) & 7;
    int it = 0;
    auto issue = [&](unsigned d) {
      int ks = it;
      if (((it - rot) & 7) < 2) { ks += P_LEAD; if (ks >= P_NSTEP) ks -= P_NSTEP; }
      const char* wp = wo + (size_t)ks * (32 * DM * 2);
      dma16<0, 0>(wp, wofs[0], d + 1024u * j); dma16<0, 4096>(wp, wofs[1], d + 1024u * j);
      if (j < 2) dma16<0, 8192>(wp, wofs[2], d + 1024u * j);
      const char* oq = op + ks * 2048;
      dma16<0, P_WOB>(oq, l16, d + 4096u * j); dma16<0, P_WOB + 1024>(oq + 1024, l16, d + 4096u * j);
      dma16<0, P_WOB + 2048>(oq + 384 * 1024, l16, d + 4096u * j); dma16<0, P_WOB + 3072>(oq + 385 * 1024, l16, d + 4096u * j);
      if (j >= 2) dma16<0, P_WOB + 16384>(o8 + ks * 1024, l16, d + 1024u * (j & 1));
      ++it;
    };
#pragma unroll
    for (int s = 0; s < P_LA; ++s) issue(lds0 + s * P_CH);
    P_BAR(21);
    int sw = P_LA * P_CH;
    for (int st = 0; st < P_NSTEP; ++st) {
      if (st + P_LA < P_NSTEP) issue(lds0 + sw);
      sw = (sw == (P_NSLOT - 1) * P_CH) ? 0 : sw + P_CH;
      if (st + P_LA < P_NSTEP) { P_BAR(21); } else if (st + P_LA == P_NSTEP) { P_BAR(14); } else if (st + P_LA == P_NSTEP + 1) { P_BAR(7); } else { P_BAR(0); }
    }
    P_BAR(0);
    P_BAR(0);
  }
#undef P_BAR
}

extern "C" void kernel_launch(void* const* d_in, const int* in_sizes, int n_in, void* d_out, int out_size, void* d_ws, size_t ws_size,
                              hipStream_t stream) {
  (void)hipFuncSetAttribute((const void*)attn_fused, hipFuncAttributeMaxDynamicSharedMemorySize, LDS_TOTAL + 3840);
  (void)hipFuncSetAttribute((const void*)oproj, hipFuncAttributeMaxDynamicSharedMemorySize, P_LDS + 4096 + 256);
  attn_fused<<<256, 512, LDS_TOTAL + 3840, stream>>>((const bf16*)d_in[0], (const bf16*)d_in[1], (const bf16*)d_in[2], (const bf16*)d_in[3],
                                              (const bf16*)d_in[5], (const bf16*)d_in[6], (const bf16*)d_in[7], (const bf16*)d_in[8], (char*)d_ws);
  oproj<<<256, 512, P_LDS + 4096 + 256, stream>>>((const char*)d_ws, (const bf16*)d_in[4], (const bf16*)d_in[9], (bf16*)d_out, (char*)d_ws + WS_FLAG);
}
```

```cpp
#include <hip/hip_runtime.h>
#include <hip/hip_bf16.h>
#include <stdint.h>
#include <utility>
#include <type_traits>

typedef __hip_bfloat16 bf16;
typedef short bf16x8 __attribute__((ext_vector_type(8)));
typedef short s16x4 __attribute__((ext_vector_type(4)));
typedef float f32x16 __attribute__((ext_vector_type(16)));
typedef float f32x4 __attribute__((ext_vector_type(4)));
typedef float f32x2 __attribute__((ext_vector_type(2)));
typedef unsigned u32x4 __attribute__((ext_vector_type(4)));
typedef __bf16 bf2_t __attribute__((ext_vector_type(2)));
typedef __attribute__((address_space(3))) char lds_char;
typedef __attribute__((address_space(3))) s16x4 lds_s16x4;
typedef __attribute__((address_space(3))) bf16x8 lds_bf16x8;

#define DI __device__ __forceinline__
#define MFMA32(a, b, c) __builtin_amdgcn_mfma_f32_32x32x16_bf16((a), (b), (c), 0, 0, 0)
#define MFMA16(a, b, c) __builtin_amdgcn_mfma_f32_16x16x32_bf16((a), (b), (c), 0, 0, 0)
#define SB() __builtin_amdgcn_sched_barrier(0)
#define PIN(x) asm volatile("" : "+v"(x))

constexpr int NB = 16, SQ = 144, DM = 640, NH = 32, SK = 512, DH = 192, HD = NH * DH;
constexpr int CHB = 12288;
constexpr int XB = 9216;
constexpr int LDS_K = 0, LDS_V = 3 * CHB, LDS_WX = 6 * CHB, WXS = CHB + XB;
constexpr int LDS_Q = LDS_WX + WXS;
constexpr int LDS_SCR = LDS_WX + 4 * WXS;
constexpr int LDS_TOTAL = LDS_SCR + 256;
constexpr float QSCALE = 0.07216878364870322f * 1.4426950408889634f;
constexpr int BND = 0;
constexpr int TPOS = 0;
constexpr int WXPOS = 0;
constexpr int NBX = 8;
constexpr int TOUCH_AHEAD = 2;
constexpr int P_PRIO = 3;
constexpr int P_SKIP = 1;
constexpr float THR = 8.0f;
constexpr size_t O_PER_B = (size_t)SQ * HD * 2;

DI unsigned cvtpk(float lo, float hi) {
  f32x2 v = {lo, hi};
  bf2_t b = __builtin_convertvector(v, bf2_t);
  return __builtin_bit_cast(unsigned, b);
}
DI bf16x8 pack8(float a0, float a1, float a2, float a3, float a4, float a5, float a6, float a7) {
  u32x4 w = {cvtpk(a0, a1), cvtpk(a2, a3), cvtpk(a4, a5), cvtpk(a6, a7)};
  return __builtin_bit_cast(bf16x8, w);
}
#define OSTORE(v, p) __builtin_nontemporal_store((v), (bf16x8*)(p))
DI s16x4 trread(const lds_char* p) { return __builtin_amdgcn_ds_read_tr16_b64_v4i16((lds_s16x4*)p); }
DI bf16x8 tr2(const lds_char* p0, const lds_char* p1) {
  s16x4 lo = trread(p0), hi = trread(p1);
  return __builtin_shufflevector(lo, hi, 0, 1, 2, 3, 4, 5, 6, 7);
}
DI bf16x8 ldsr(const lds_char* p) { return *(const lds_bf16x8*)p; }

#define KVFLAGS "nt"
template <int NT, int IMM = 0>
DI void dma16(const char* sbase, unsigned voff, unsigned ldsdst) {
  unsigned keep;
  if constexpr (NT)
    asm volatile("s_mov_b32 %0, m0\n\ts_add_u32 m0, %3, %4\n\ts_nop 0\n\tglobal_load_lds_dwordx4 %1, %2 " KVFLAGS "\n\ts_mov_b32 m0, %0"
                 : "=&s"(keep) : "v"(voff), "s"(sbase), "s"(ldsdst), "i"(IMM) : "memory", "scc");
  else
    asm volatile("s_mov_b32 %0, m0\n\ts_add_u32 m0, %3, %4\n\ts_nop 0\n\tglobal_load_lds_dwordx4 %1, %2\n\ts_mov_b32 m0, %0"
                 : "=&s"(keep) : "v"(voff), "s"(sbase), "s"(ldsdst), "i"(IMM) : "memory", "scc");
}
DI void touch4(const char* sbase, unsigned voff, unsigned ldsdst) {
  unsigned keep;
  asm volatile("s_mov_b32 %0, m0\n\ts_mov_b32 m0, %3\n\ts_nop 0\n\tglobal_load_lds_dword %1, %2\n\ts_mov_b32 m0, %0"
               : "=&s"(keep) : "v"(voff), "s"(sbase), "s"(ldsdst) : "memory");
}
#define WAIT_BAR(N) do { SB(); asm volatile("s_waitcnt vmcnt(" #N ") lgkmcnt(0)\n\ts_barrier" ::: "memory"); SB(); } while (0)

DI unsigned swv(unsigned row) { return (((row >> 1) & 1) << 2) | (((row >> 2) & 1) << 1); }
DI float max3(float a, float b, float c) { return __builtin_fmaxf(__builtin_fmaxf(a, b), c); }
DI float xhalf_max(float m) {
  auto rr = __builtin_amdgcn_permlane32_swap(__float_as_uint(m), __float_as_uint(m), false, false);
  return __builtin_fmaxf(__uint_as_float(rr[0]), __uint_as_float(rr[1]));
}
DI float xhalf_sum(float m) {
  auto rr = __builtin_amdgcn_permlane32_swap(__float_as_uint(m), __float_as_uint(m), false, false);
  return __uint_as_float(rr[0]) + __uint_as_float(rr[1]);
}
DI float x16_max(float m) {
  auto rr = __builtin_amdgcn_permlane16_swap(__float_as_uint(m), __float_as_uint(m), false, false);
  return __builtin_fmaxf(__uint_as_float(rr[0]), __uint_as_float(rr[1]));
}
DI float x16_sum(float m) {
  auto rr = __builtin_amdgcn_permlane16_swap(__float_as_uint(m), __float_as_uint(m), false, false);
  return __uint_as_float(rr[0]) + __uint_as_float(rr[1]);
}
template <int N, class F, int... Is> DI void static_for_impl(F&& f, std::integer_sequence<int, Is...>) { (f(std::integral_constant<int, Is>{}), ...); }
template <int N, class F> DI void static_for(F&& f) { static_for_impl<N>(f, std::make_integer_sequence<int, N>{}); }
DI int inc3(int x) { return x == 2 * CHB ? 0 : x + CHB; }

struct Ctx {
  const char* kg;
  const char* vg;
  const char* wq;
  const char* xg;
  char* og;
  int h0;
  int b, xs;
};

DI void main_wave(const Ctx& c, lds_char* lds, int w, int lane) {
  const unsigned lds0 = (unsigned)(uintptr_t)lds;
  const int r32 = lane & 31, h2 = lane >> 5;
  const int wi = w > 0 ? w - 1 : 0;
  const bool dm = w > 0;
  unsigned koff[2], voff;
#pragma unroll
  for (int ii = 0; ii < 2; ++ii) {
    unsigned p = 64u * (2 * wi + ii) + lane, row = (p >> 3) / 3;
    koff[ii] = 16u * (p ^ ((row >> 1) & 7));
  }
  { unsigned p = 64u * wi + lane, row = (p >> 3) / 3; voff = 16u * (p ^ swv(row)); }
  const unsigned dk = lds0 + LDS_K + 2048u * wi, dv = lds0 + LDS_V + 1024u * wi;
  unsigned kb[4];
#pragma unroll
  for (int s = 0; s < 4; ++s) kb[s] = LDS_K + 384u * r32 + 16u * ((2u * s + h2) ^ ((r32 >> 1) & 7));
  unsigned vb[2];
  {
    const unsigned blk = (lane >> 4) & 1, q4 = (lane & 15) >> 2, p = lane & 3;
#pragma unroll
    for (int t = 0; t < 2; ++t)
      vb[t] = LDS_V + 384u * (4u * h2 + q4) + 16u * (4u * (t ^ (q4 >> 1)) + 2u * (blk ^ h2) + (p >> 1)) + 8u * (p & 1);
  }

  bf16x8 qf[12];
  f32x16 o[6];
#pragma unroll
  for (int t = 0; t < 6; ++t) o[t] = f32x16{};
  float m = 0.f, mc = 0.f, l = 0.f, alpha = 1.f;
  bool resc = false;
  bf16x8 pf0 = {}, pf1 = {};
  bf16x8 f0 = {}, f1 = {}, f2 = {}, f3 = {}, f4 = {}, f5 = {};
  f32x16 sA = {}, sB = {};
  int ks_rd = 0;
  int vs_rd = 0;
  const char* kptr = c.kg;
  const char* vptr = c.vg;
  int ks_wr = 0, vs_wr = 0;

#define KRD(ks) ldsr(lds + ks_rd + kb[(ks) & 3] + 128 * ((ks) >> 2))
#define VRD(j) tr2(lds + vs_rd + vb[((j) % 6) & 1] + 128 * (((j) % 6) >> 1) + 6144 * ((j) / 6), lds + vs_rd + vb[((j) % 6) & 1] + 128 * (((j) % 6) >> 1) + 6144 * ((j) / 6) + 3072)
#define DMAK4() do { if (dm) { dma16<1, 0>(kptr, koff[0], dk + ks_wr); dma16<1, 1024>(kptr, koff[1], dk + ks_wr); \
    dma16<1, 6144>(kptr + 6144, koff[0], dk + ks_wr); dma16<1, 7168>(kptr + 6144, koff[1], dk + ks_wr); } } while (0)
#define DMAV4P(vp_, d_) do { if (dm) { dma16<1, 0>(vp_, voff, d_); dma16<1, 3072>((vp_) + 3072, voff, d_); \
    dma16<1, 6144>((vp_) + 6144, voff, d_); dma16<1, 9216>((vp_) + 9216, voff, d_); } } while (0)
#define DMAV4() DMAV4P(vptr, dv + vs_wr)
#define ADVK() do { kptr += CHB; ks_wr = inc3(ks_wr); } while (0)
#define ADVV() do { vptr += CHB; vs_wr = inc3(vs_wr); } while (0)
#define EXP2M(x) __builtin_amdgcn_exp2f(__builtin_fmaf((x), QSCALE, mc))

  auto rescale = [&]() {
    asm volatile("" ::: "memory");
    if (__builtin_expect(resc, 0)) {
#pragma unroll
      for (int tt = 0; tt < 6; ++tt)
#pragma unroll
        for (int r = 0; r < 16; ++r) o[tt][r] *= alpha;
      resc = false;
    }
  };
  auto decide = [&](float mx) {
    asm volatile("" ::: "memory");
    if (__builtin_expect(__any(mx - m > THR / QSCALE), 0)) {
      float mn = __builtin_fmaxf(m, mx);
      alpha = __builtin_amdgcn_exp2f((m - mn) * QSCALE);
      m = mn; mc = -mn * QSCALE; l *= alpha; resc = true;
    }
  };
  auto step = [&](f32x16& sN, f32x16& sP, bool gk, bool gv) {
    o[0] = MFMA32(f0, pf1, o[0]); f0 = KRD(0); sP[6] = EXP2M(sP[6]); sP[7] = EXP2M(sP[7]); PIN(sP); SB();
    o[1] = MFMA32(f1, pf1, o[1]); f1 = KRD(1); sP[8] = EXP2M(sP[8]); sP[9] = EXP2M(sP[9]); PIN(sP); SB();
    o[2] = MFMA32(f2, pf1, o[2]); f2 = KRD(2); sP[10] = EXP2M(sP[10]); sP[11] = EXP2M(sP[11]); PIN(sP); SB();
    o[3] = MFMA32(f3, pf1, o[3]); f3 = KRD(3); sP[12] = EXP2M(sP[12]); sP[13] = EXP2M(sP[13]); PIN(sP); SB();
    o[4] = MFMA32(f4, pf1, o[4]); f4 = KRD(4); sP[14] = EXP2M(sP[14]); sP[15] = EXP2M(sP[15]); PIN(sP); SB();
    o[5] = MFMA32(f5, pf1, o[5]); f5 = KRD(5); pf0 = pack8(sP[0], sP[1], sP[2], sP[3], sP[4], sP[5], sP[6], sP[7]); PIN(pf0); SB();
    float sum;
    sN = MFMA32(f0, qf[0], (f32x16{})); f0 = KRD(6); sum = sP[0] + sP[1]; sum += sP[2]; sum += sP[3]; PIN(sum); SB();
    sN = MFMA32(f1, qf[1], sN); f1 = KRD(7); pf1 = pack8(sP[8], sP[9], sP[10], sP[11], sP[12], sP[13], sP[14], sP[15]); PIN(pf1); SB();
    sN = MFMA32(f2, qf[2], sN); f2 = KRD(8); sum += sP[4]; sum += sP[5]; sum += sP[6]; sum += sP[7]; PIN(sum); SB();
    sN = MFMA32(f3, qf[3], sN); f3 = KRD(9); sum += sP[8]; sum += sP[9]; sum += sP[10]; sum += sP[11]; PIN(sum); SB();
    sN = MFMA32(f4, qf[4], sN); f4 = KRD(10); SB();
    sN = MFMA32(f5, qf[5], sN); f5 = KRD(11); sum += sP[12]; sum += sP[13]; sum += sP[14]; sum += sP[15]; l += sum; PIN(l); SB();
    rescale();
    SB();
    sN = MFMA32(f0, qf[6], sN); f0 = VRD(0); SB();
    sN = MFMA32(f1, qf[7], sN); f1 = VRD(1); SB();
    sN = MFMA32(f2, qf[8], sN); f2 = VRD(2); SB();
    sN = MFMA32(f3, qf[9], sN); f3 = VRD(3); SB();
    sN = MFMA32(f4, qf[10], sN); f4 = VRD(4); SB();
    sN = MFMA32(f5, qf[11], sN); f5 = VRD(5); SB();
    o[0] = MFMA32(f0, pf0, o[0]); f0 = VRD(6); SB();
    o[1] = MFMA32(f1, pf0, o[1]); f1 = VRD(7); SB();
    float mx = max3(sN[0], sN[1], sN[2]), mx2 = max3(sN[3], sN[4], sN[5]);
    mx = max3(mx, sN[6], sN[7]); mx2 = max3(mx2, sN[8], sN[9]);
    o[2] = MFMA32(f2, pf0, o[2]); f2 = VRD(8);
    mx = max3(mx, sN[10], sN[11]); mx2 = max3(mx2, sN[12], sN[13]); mx = max3(mx, sN[14], sN[15]); mx = __builtin_fmaxf(mx, mx2);
    mx = xhalf_max(mx);
    SB();
    decide(mx);
    SB();
    o[3] = MFMA32(f3, pf0, o[3]); f3 = VRD(9); sN[0] = EXP2M(sN[0]); sN[1] = EXP2M(sN[1]); PIN(sN); SB();
    o[4] = MFMA32(f4, pf0, o[4]); f4 = VRD(10); sN[2] = EXP2M(sN[2]); sN[3] = EXP2M(sN[3]); PIN(sN); SB();
    o[5] = MFMA32(f5, pf0, o[5]); f5 = VRD(11); sN[4] = EXP2M(sN[4]); sN[5] = EXP2M(sN[5]); PIN(sN); SB();
    if (gk) { DMAK4(); }
    if (gv) { DMAV4(); }
    if (gk) ADVK();
    if (gv) ADVV();
    ks_rd = inc3(ks_rd); vs_rd = inc3(vs_rd);
  };
  auto readq = [&]() {
    unsigned qb = LDS_Q + w * 12288 + lane * 16;
    PIN(qb);
#pragma unroll
    for (int ks = 0; ks < 12; ++ks) qf[ks] = ldsr(lds + qb + ks * 1024);
  };
  auto newhead = [&](auto epi_, float inv, char* op) {
    constexpr bool EPI = decltype(epi_)::value;
    f0 = KRD(0); f1 = KRD(1); f2 = KRD(2); f3 = KRD(3); f4 = KRD(4); f5 = KRD(5);
    SB();
    static_for<12>([&](auto k_) {
      constexpr int k = decltype(k_)::value;
      bf16x8& f = (k % 6 == 0) ? f0 : (k % 6 == 1) ? f1 : (k % 6 == 2) ? f2 : (k % 6 == 3) ? f3 : (k % 6 == 4) ? f4 : f5;
      if constexpr (k == 0) sA = MFMA32(f, qf[0], (f32x16{})); else sA = MFMA32(f, qf[k], sA);
      if constexpr (k < 6) f = KRD(k + 6); else f = bf16x8{};
      if constexpr (EPI) {
        constexpr int tt = k >> 1, sp = k & 1;
        bf16x8 v = pack8(o[tt][8 * sp] * inv, o[tt][8 * sp + 1] * inv, o[tt][8 * sp + 2] * inv, o[tt][8 * sp + 3] * inv,
                         o[tt][8 * sp + 4] * inv, o[tt][8 * sp + 5] * inv, o[tt][8 * sp + 6] * inv, o[tt][8 * sp + 7] * inv);
        OSTORE(v, (op + k * 1024) + (unsigned)(lane * 16));
        if constexpr (sp == 1) { o[tt] = f32x16{}; PIN(o[tt]); }
      }
      SB();
    });
    float mx = max3(sA[0], sA[1], sA[2]);
    mx = max3(mx, sA[3], sA[4]); mx = max3(mx, sA[5], sA[6]); mx = max3(mx, sA[7], sA[8]); mx = max3(mx, sA[9], sA[10]);
    mx = max3(mx, sA[11], sA[12]); mx = max3(mx, sA[13], sA[14]); mx = __builtin_fmaxf(mx, sA[15]);
    m = xhalf_max(mx); mc = -m * QSCALE; l = 0.f; resc = false;
#pragma unroll
    for (int r = 0; r < 6; ++r) sA[r] = EXP2M(sA[r]);
    PIN(sA);
    ks_rd = inc3(ks_rd);
  };

  {
    unsigned wo[3], xo[3];
#pragma unroll
    for (int ii = 0; ii < 3; ++ii) {
      unsigned pp = 64u * (3 * w + ii) + lane, row = (pp >> 3) / 3, c16 = (pp ^ swv(row)) - 24u * row;
      wo[ii] = row * (HD * 2) + 16u * c16;
      unsigned r = 16u * (w + 4 * ii) + (lane >> 2);
      xo[ii] = r * (DM * 2) + 16u * ((lane & 3) ^ ((r >> 2) & 3));
    }
    auto pair_issue = [&](int p) {
      const int pr = (p + 1) % 3;
      const unsigned base = lds0 + (pr == 2 ? 0 : LDS_WX + 2 * pr * WXS);
      const unsigned bw = base + 3072u * w, bx = base + CHB + 1024u * w;
      const char* sw = c.wq + (size_t)p * (64 * HD * 2);
      const char* sx = c.xg + p * 128;
      dma16<0, 0>(sw, wo[0], bw); dma16<0, 1024>(sw, wo[1], bw); dma16<0, 2048>(sw, wo[2], bw);
      dma16<0, 0>(sx, xo[0], bx); dma16<0, 4096>(sx, xo[1], bx); if (w == 0) dma16<0, 8192>(sx, xo[2], bx);
      sw += 32 * HD * 2; sx += 64;
      dma16<0, WXS>(sw, wo[0], bw); dma16<0, WXS + 1024>(sw, wo[1], bw); dma16<0, WXS + 2048>(sw, wo[2], bw);
      dma16<0, WXS>(sx, xo[0], bx); dma16<0, WXS + 4096>(sx, xo[1], bx); if (w == 0) dma16<0, WXS + 8192>(sx, xo[2], bx);
    };
    pair_issue(0); pair_issue(1);
    WAIT_BAR(10);
    for (int p = 0; p < 8; ++p) {
      pair_issue(p + 2);
      if (p >= 6) {
        const char* vp = c.vg + (size_t)(p - 5) * CHB; const unsigned d = dv + (p - 5) * CHB;
        DMAV4P(vp, d);
        WAIT_BAR(10);
      } else { WAIT_BAR(10); }
    }
    DMAK4(); ADVK(); DMAV4();
    DMAK4(); ADVK();
    vptr += 3 * CHB;
    if (dm) { WAIT_BAR(12); } else { WAIT_BAR(0); }
    DMAK4(); ADVK();
    WAIT_BAR(12);
    WAIT_BAR(63);
    readq();
    WAIT_BAR(63);
    newhead(std::false_type{}, 0.f, nullptr);
    WAIT_BAR(4);
  }

  for (int e = 0; e < 4; ++e) {
    const bool last = (e == 3);
    step(sB, sA, true, e > 0);
    if (e == 0) { WAIT_BAR(4); } else { WAIT_BAR(20); }
    for (int i = 1; i < 13; i += 2) {
      step(sA, sB, true, true); WAIT_BAR(8);
      step(sB, sA, true, true); WAIT_BAR(8);
    }
    step(sA, sB, !last, true);  if (last) { WAIT_BAR(4); } else { WAIT_BAR(8); }
    step(sB, sA, !last, !last); if (last) { WAIT_BAR(0); } else { WAIT_BAR(8); }
    {
      if (!last) { DMAK4(); ADVK(); DMAV4(); ADVV(); }
      SB();
      o[0] = MFMA32(f0, pf1, o[0]); f0 = VRD(0); sB[6] = EXP2M(sB[6]); sB[7] = EXP2M(sB[7]); PIN(sB); SB();
      o[1] = MFMA32(f1, pf1, o[1]); f1 = VRD(1); sB[8] = EXP2M(sB[8]); sB[9] = EXP2M(sB[9]); PIN(sB); SB();
      o[2] = MFMA32(f2, pf1, o[2]); f2 = VRD(2); pf0 = pack8(sB[0], sB[1], sB[2], sB[3], sB[4], sB[5], sB[6], sB[7]); PIN(pf0); SB();
      o[3] = MFMA32(f3, pf1, o[3]); f3 = VRD(3); sB[10] = EXP2M(sB[10]); sB[11] = EXP2M(sB[11]); PIN(sB); SB();
      o[4] = MFMA32(f4, pf1, o[4]); f4 = VRD(4); sB[12] = EXP2M(sB[12]); sB[13] = EXP2M(sB[13]); PIN(sB); SB();
      o[5] = MFMA32(f5, pf1, o[5]); f5 = VRD(5); sB[14] = EXP2M(sB[14]); sB[15] = EXP2M(sB[15]); PIN(sB); SB();
      rescale();
      SB();
      float sum;
      o[0] = MFMA32(f0, pf0, o[0]); f0 = VRD(6); pf1 = pack8(sB[8], sB[9], sB[10], sB[11], sB[12], sB[13], sB[14], sB[15]); PIN(pf1); SB();
      o[1] = MFMA32(f1, pf0, o[1]); f1 = VRD(7); sum = sB[0] + sB[1]; sum += sB[2]; sum += sB[3]; PIN(sum); SB();
      o[2] = MFMA32(f2, pf0, o[2]); f2 = VRD(8); sum += sB[4]; sum += sB[5]; sum += sB[6]; sum += sB[7]; PIN(sum); SB();
      o[3] = MFMA32(f3, pf0, o[3]); f3 = VRD(9); sum += sB[8]; sum += sB[9]; sum += sB[10]; sum += sB[11]; PIN(sum); SB();
      o[4] = MFMA32(f4, pf0, o[4]); f4 = VRD(10); sum += sB[12]; sum += sB[13]; sum += sB[14]; sum += sB[15]; l += sum; PIN(l); SB();
      o[5] = MFMA32(f5, pf0, o[5]); f5 = VRD(11); SB();
      float inv = 1.0f / xhalf_sum(l);
      o[0] = MFMA32(f0, pf1, o[0]); o[1] = MFMA32(f1, pf1, o[1]); o[2] = MFMA32(f2, pf1, o[2]);
      o[3] = MFMA32(f3, pf1, o[3]); o[4] = MFMA32(f4, pf1, o[4]); o[5] = MFMA32(f5, pf1, o[5]);
      PIN(inv);
      vs_rd = inc3(vs_rd);
      char* op = c.og + (size_t)(w * 384 + (c.h0 + e) * 12) * 1024;
      if (!last) {
        WAIT_BAR(63);
        readq();
        WAIT_BAR(63);
      }
      newhead(std::true_type{}, inv, op);
      if (last) break;
      WAIT_BAR(20);
    }
  }
#undef KRD
#undef VRD
#undef DMAK4
#undef DMAV4
#undef DMAV4P
#undef ADVK
#undef ADVV
#undef EXP2M
}

struct WxDma {
  unsigned woff[4], xoff[3];
  unsigned dw, dx;
  DI void init(unsigned lds0, int j, int lane) {
#pragma unroll
    for (int ii = 0; ii < 4; ++ii) {
      unsigned p = 64u * (4 * (j - 1) + ii) + lane, row = (p >> 3) / 3;
      unsigned cc = (p ^ swv(row)) - 24u * row;
      woff[ii] = row * (HD * 2) + 16u * cc;
    }
#pragma unroll
    for (int ii = 0; ii < 3; ++ii) {
      unsigned xi = 3u * (j - 1) + ii;
      unsigned r = 16u * xi + (lane >> 2);
      xoff[ii] = r * (DM * 2) + 16u * ((lane & 3) ^ ((r >> 2) & 3));
    }
    dw = lds0 + LDS_WX + 4096u * (j - 1); dx = lds0 + LDS_WX + CHB + 3072u * (j - 1);
  }
  template <int II> DI void w(const Ctx& c, int e, int ch, int slot) const {
    dma16<0, 1024 * II>(c.wq + (size_t)ch * (32 * HD * 2) + e * (DH * 2), woff[II], dw + slot * WXS);
  }
  template <int II> DI void x(const Ctx& c, int ch, int slot) const { dma16<0, 1024 * II>(c.xg + ch * 64, xoff[II], dx + slot * WXS); }
};

DI void proj_wave(const Ctx& c, lds_char* lds, int j, int lane) {
  const unsigned lds0 = (unsigned)(uintptr_t)lds;
  const int r32 = lane & 31, h2 = lane >> 5;
  WxDma dma; dma.init(lds0, j, lane);
  unsigned wb[2][2];
  {
    const unsigned blk = (lane >> 4) & 1, q4 = (lane & 15) >> 2, p = lane & 3;
#pragma unroll
    for (int tt = 0; tt < 2; ++tt)
#pragma unroll
      for (int a = 0; a < 2; ++a)
        wb[tt][a] = LDS_WX + 384u * (8u * h2 + 4u * a + q4) + 128u * (j - 1) +
                    16u * (4u * (tt ^ (q4 >> 1)) + 2u * (blk ^ a) + (p & 1)) + 8u * (p >> 1);
  }
  unsigned xb[2];
#pragma unroll
  for (int sp = 0; sp < 2; ++sp) xb[sp] = LDS_WX + CHB + 64u * r32 + 16u * ((2u * sp + h2) ^ ((r32 >> 2) & 3));
  f32x16 acc[4][2];
  bf16x8 aA[2], bA[4], aB[2], bB[4];
#define KSTEP(ar, br, slot, sp, am, bm, D0, D1, D2, D3, D4, D5, D6) do { \
    const lds_char* bs_ = lds + (slot) * WXS; \
    acc[0][0] = MFMA32(am[0], bm[0], acc[0][0]); ar[0] = tr2(bs_ + wb[0][0] + 6144 * (sp), bs_ + wb[0][1] + 6144 * (sp)); SB(); \
    acc[0][1] = MFMA32(am[1], bm[0], acc[0][1]); br[0] = ldsr(bs_ + xb[sp]); D0; SB(); \
    acc[1][0] = MFMA32(am[0], bm[1], acc[1][0]); ar[1] = tr2(bs_ + wb[1][0] + 6144 * (sp), bs_ + wb[1][1] + 6144 * (sp)); D1; SB(); \
    acc[1][1] = MFMA32(am[1], bm[1], acc[1][1]); br[1] = ldsr(bs_ + xb[sp] + 2048); D2; SB(); \
    acc[2][0] = MFMA32(am[0], bm[2], acc[2][0]); br[2] = ldsr(bs_ + xb[sp] + 4096); D3; SB(); \
    acc[2][1] = MFMA32(am[1], bm[2], acc[2][1]); br[3] = ldsr(bs_ + xb[sp] + 6144); D4; SB(); \
    acc[3][0] = MFMA32(am[0], bm[3], acc[3][0]); D5; SB(); \
    acc[3][1] = MFMA32(am[1], bm[3], acc[3][1]); D6; SB(); } while (0)
#define DW(e_, ch_, sl_, ii) dma.template w<ii>(c, e_, ch_, sl_)
#define DX(ch_, sl_, ii) dma.template x<ii>(c, ch_, sl_)
#define NOP (void)0
#define CH6(e_, ch_, sl_) DW(e_, ch_, sl_, 0), DW(e_, ch_, sl_, 1), DW(e_, ch_, sl_, 2), DW(e_, ch_, sl_, 3), DX(ch_, sl_, 0), DX(ch_, sl_, 1), DX(ch_, sl_, 2)
#define CH6C(cond, e_, ch_, sl_) if (cond) DW(e_, ch_, sl_, 0), if (cond) DW(e_, ch_, sl_, 1), if (cond) DW(e_, ch_, sl_, 2), if (cond) DW(e_, ch_, sl_, 3), if (cond) DX(ch_, sl_, 0), if (cond) DX(ch_, sl_, 1), if (cond) DX(ch_, sl_, 2)
#define KSTEP_(...) KSTEP(__VA_ARGS__)
#define KS(ar, br, slot, sp, am, bm) KSTEP(ar, br, slot, sp, am, bm, NOP, NOP, NOP, NOP, NOP, NOP, NOP)
#define CH7(e_, ch_, sl_) do { DW(e_, ch_, sl_, 0); DW(e_, ch_, sl_, 1); DW(e_, ch_, sl_, 2); DW(e_, ch_, sl_, 3); DX(ch_, sl_, 0); DX(ch_, sl_, 1); DX(ch_, sl_, 2); } while (0)

  auto zero = [&]() {
#pragma unroll
    for (int w = 0; w < 4; ++w) { acc[w][0] = f32x16{}; acc[w][1] = f32x16{}; bB[w] = bf16x8{}; }
    aB[0] = bf16x8{}; aB[1] = bf16x8{};
  };
  auto handoff = [&]() {
#pragma unroll
    for (int w = 0; w < 4; ++w) { acc[w][0] = MFMA32(aB[0], bB[w], acc[w][0]); acc[w][1] = MFMA32(aB[1], bB[w], acc[w][1]); }
#pragma unroll
    for (int w = 0; w < 4; ++w)
#pragma unroll
      for (int tt = 0; tt < 2; ++tt)
#pragma unroll
        for (int sp = 0; sp < 2; ++sp) {
          const f32x16& a = acc[w][tt];
          bf16x8 f = pack8(a[8 * sp], a[8 * sp + 1], a[8 * sp + 2], a[8 * sp + 3], a[8 * sp + 4], a[8 * sp + 5], a[8 * sp + 6], a[8 * sp + 7]);
          *(lds_bf16x8*)(lds + LDS_Q + (w * 12 + 4 * (j - 1) + 2 * tt + sp) * 1024 + lane * 16) = f;
        }
  };
  zero();
  WAIT_BAR(63);
  for (int p = 0; p < 10; ++p) {
    const int pr = (p + 1) % 3;
    lds_char* const lsave = lds;
    lds = lsave + (pr == 2 ? -LDS_WX : 2 * pr * WXS);
    KS(aA, bA, 0, 0, aB, bB);
    KS(aB, bB, 0, 1, aA, bA);
    KS(aA, bA, 1, 0, aB, bB);
    KS(aB, bB, 1, 1, aA, bA);
    lds = lsave;
    if (p == 9) CH7(1, 0, 0);
    WAIT_BAR(63);
  }
  for (int e = 1; e <= 4; ++e) {
    handoff();
    WAIT_BAR(63);
    WAIT_BAR(63);
    if (e == 4) { WAIT_BAR(63); break; }
    CH7(e, 1, 1); CH7(e, 2, 2); CH7(e, 3, 3);
    zero();
    WAIT_BAR(21);
    for (int mm = 0; mm < 5; ++mm) {
      const int c4 = 4 * mm;
      const bool lastm = (mm == 4);
      if (WXPOS == 0 && (BND || mm > 0)) { CH7(e, c4 + 2, 2); CH7(e, c4 + 3, 3); }
      KS(aA, bA, 0, 0, aB, bB);
      KS(aB, bB, 0, 1, aA, bA);
      if (WXPOS == 1 && (BND || mm > 0)) { CH7(e, c4 + 2, 2); CH7(e, c4 + 3, 3); }
      WAIT_BAR(14);
      if (WXPOS == 0 && !lastm) CH7(e, c4 + 4, 0);
      KS(aA, bA, 1, 0, aB, bB);
      KS(aB, bB, 1, 1, aA, bA);
      if (WXPOS == 1 && !lastm) CH7(e, c4 + 4, 0);
      if (lastm) { WAIT_BAR(0); } else { WAIT_BAR(7); }
      {
        const int e2 = lastm ? e + 1 : e, ch2 = lastm ? 0 : c4 + 5, sl2 = lastm ? 0 : 1;
        if (WXPOS == 0 && e2 < 4) CH7(e2, ch2, sl2);
        KS(aA, bA, 2, 0, aB, bB);
        KS(aB, bB, 2, 1, aA, bA);
        KS(aA, bA, 3, 0, aB, bB);
        KS(aB, bB, 3, 1, aA, bA);
        if (WXPOS == 1 && e2 < 4) CH7(e2, ch2, sl2);
        WAIT_BAR(7);
      }
    }
  }
  for (int i = 0; i < 15; ++i) {
    WAIT_BAR(63);
  }
#undef KSTEP
#undef KSTEP_
#undef CH6
#undef CH6C
#undef DX
#undef DW
#undef NOP
}

DI void q8_wave(const Ctx& c, lds_char* lds, int lane) {
  const unsigned lds0 = (unsigned)(uintptr_t)lds;
  const unsigned r16 = lane & 15, g = lane >> 4, q4 = (lane & 15) >> 2, p = lane & 3;
  unsigned wb[2][2];
#pragma unroll
  for (int s1 = 0; s1 < 2; ++s1)
#pragma unroll
    for (int a = 0; a < 2; ++a)
      wb[s1][a] = LDS_WX + 384u * (8u * g + 4u * a + q4) + 16u * (4u * (s1 ^ (q4 >> 1)) + (p ^ (a << 1)));
  const unsigned xb = LDS_WX + CHB + 64u * (128u + r16) + 16u * (g ^ ((r16 >> 2) & 3));
  unsigned kb[2];
#pragma unroll
  for (int s1 = 0; s1 < 2; ++s1) kb[s1] = LDS_K + 384u * r16 + 16u * ((4u * s1 + g) ^ ((r16 >> 1) & 7));
  unsigned vb[4];
#pragma unroll
  for (int T = 0; T < 4; ++T)
    vb[T] = LDS_V + 384u * (4u * g + q4) + 16u * (4u * ((T >> 1) ^ (q4 >> 1)) + 2u * ((T & 1) ^ (g & 1)) + (p >> 1)) + 8u * (p & 1);

  constexpr int TR = 32 / NBX, TL = 3 * TR;
  constexpr int TCH = (64 / TL) < 4 ? (64 / TL) : 4;
  unsigned toff;
  { const unsigned ll = lane < TL * TCH ? lane : TL * TCH - 1; toff = ((ll / TL) * 32u + TR * c.b + (ll % TL) / 3) * (HD * 2) + ((ll % TL) % 3) * 128u; }
  auto touchw = [&](int e, int ch) {
#pragma unroll
    for (int q = 0; q < 4 / TCH; ++q) touch4(c.wq + (size_t)(ch + q * TCH) * (32 * HD * 2) + e * (DH * 2), toff, lds0 + LDS_SCR);
  };
  constexpr int XS = 32 / NBX;
  constexpr int XR = SQ / XS;
  constexpr int XI = (2 * XR + 63) / 64;
  unsigned xtoff[XI];
#pragma unroll
  for (int q = 0; q < XI; ++q) { unsigned ll = q * 64 + lane; if (ll >= 2 * XR) ll = 2 * XR - 1; xtoff[q] = (XR * c.xs + (ll >> 1)) * (DM * 2) + (ll & 1) * 128u; }
  auto touchx0 = [&](int ch) {
#pragma unroll
    for (int q = 0; q < XI; ++q) touch4(c.xg + ch * 64, xtoff[q], lds0 + LDS_SCR);
  };
  auto touchx = [&](int ch) {
  };
#pragma unroll
  for (int g4 = 0; g4 < 5; ++g4) { touchw(0, 4 * g4); touchx0(4 * g4); }
  f32x4 qa[12], o[12];
  bf16x8 qf[6];
#pragma unroll
  for (int T = 0; T < 12; ++T) { o[T] = f32x4{}; qa[T] = f32x4{}; }
  float m = 0.f, mc = 0.f, l = 0.f, alpha = 1.f;
  bool resc = false;
  bf16x8 pf = {};
  bf16x8 fb[8] = {};
  f32x4 p0 = {}, p1 = {};
  int ks_rd = 0, vs_rd = 0;

#define KRD(i) ldsr(lds + ks_rd + kb[((i) >> 1) & 1] + 128 * ((i) >> 2) + 6144 * ((i) & 1))
#define VRD(T) tr2(lds + vs_rd + vb[(T) & 3] + 128 * ((T) >> 2), lds + vs_rd + vb[(T) & 3] + 128 * ((T) >> 2) + 6144)
#define WRD(slot, T) tr2(lds + (slot) * WXS + wb[((T) >> 1) & 1][0] + 128 * ((T) >> 2) + 8 * ((T) & 1), lds + (slot) * WXS + wb[((T) >> 1) & 1][1] + 128 * ((T) >> 2) + 8 * ((T) & 1))
#define EXP2M(x) __builtin_amdgcn_exp2f(__builtin_fmaf((x), QSCALE, mc))

  auto rescale = [&]() {
    asm volatile("" ::: "memory");
    if (__builtin_expect(resc, 0)) {
#pragma unroll
      for (int T = 0; T < 12; ++T)
#pragma unroll
        for (int r = 0; r < 4; ++r) o[T][r] *= alpha;
      resc = false;
    }
  };
  auto decide = [&](float mx) {
    asm volatile("" ::: "memory");
    if (__builtin_expect(__any(mx - m > THR / QSCALE), 0)) {
      float mn = __builtin_fmaxf(m, mx);
      alpha = __builtin_amdgcn_exp2f((m - mn) * QSCALE);
      m = mn; mc = -mn * QSCALE; l *= alpha; resc = true;
    }
  };
  auto chunk = [&](int slot) {
    bf16x8 b = ldsr(lds + slot * WXS + xb);
    bf16x8 a0 = WRD(slot, 0), a1 = WRD(slot, 1), a2 = WRD(slot, 2), a3 = WRD(slot, 3);
    SB();
    qa[0] = MFMA16(a0, b, qa[0]); a0 = WRD(slot, 4); SB();
    qa[1] = MFMA16(a1, b, qa[1]); a1 = WRD(slot, 5); SB();
    qa[2] = MFMA16(a2, b, qa[2]); a2 = WRD(slot, 6); SB();
    qa[3] = MFMA16(a3, b, qa[3]); a3 = WRD(slot, 7); SB();
    qa[4] = MFMA16(a0, b, qa[4]); a0 = WRD(slot, 8); SB();
    qa[5] = MFMA16(a1, b, qa[5]); a1 = WRD(slot, 9); SB();
    qa[6] = MFMA16(a2, b, qa[6]); a2 = WRD(slot, 10); SB();
    qa[7] = MFMA16(a3, b, qa[7]); a3 = WRD(slot, 11); SB();
    qa[8] = MFMA16(a0, b, qa[8]); qa[9] = MFMA16(a1, b, qa[9]); qa[10] = MFMA16(a2, b, qa[10]); qa[11] = MFMA16(a3, b, qa[11]); SB();
  };
  auto stepq = [&](auto nch_, auto sl_) {
    constexpr int NCH = decltype(nch_)::value, SL = decltype(sl_)::value;
    constexpr int NOPS = 24 + 12 * NCH, PV0 = 20 + 12 * NCH;
    f32x4 s0, s1; bf16x8 bx0, bx1; float mx;
    static_for<NOPS>([&](auto k_) {
      constexpr int k = decltype(k_)::value, b = k & 7;
      if constexpr (k < 8) { o[4 + k] = MFMA16(fb[b], pf, o[4 + k]); }
      else if constexpr (k < 20) {
        constexpr int i = k - 8;
        if constexpr (i == 0) s0 = MFMA16(fb[b], qf[0], (f32x4{}));
        else if constexpr (i == 1) s1 = MFMA16(fb[b], qf[0], (f32x4{}));
        else if constexpr (i & 1) s1 = MFMA16(fb[b], qf[i >> 1], s1);
        else s0 = MFMA16(fb[b], qf[i >> 1], s0);
      } else if constexpr (k < PV0) {
        constexpr int T = (k - 20) % 12, cc = (k - 20) / 12;
        qa[T] = MFMA16(fb[b], (cc ? bx1 : bx0), qa[T]);
      } else { o[k - PV0] = MFMA16(fb[b], pf, o[k - PV0]); }
      constexpr int n = k + 8;
      if constexpr (n < 20) fb[b] = KRD(n - 8);
      else if constexpr (n < PV0) fb[b] = WRD(SL + (n - 20) / 12, (n - 20) % 12);
      else if constexpr (n < NOPS) fb[b] = VRD(n - PV0);
      else fb[b] = VRD(4 + b);
      if constexpr (k == 2 && NCH >= 1) bx0 = ldsr(lds + SL * WXS + xb);
      if constexpr (k == 4 && NCH >= 2) bx1 = ldsr(lds + (SL + 1) * WXS + xb);
      if constexpr (k == 5) { float sum = (p0[0] + p0[1]) + (p0[2] + p0[3]) + (p1[0] + p1[1]) + (p1[2] + p1[3]); l += sum; PIN(l); }
      if constexpr (k == 9) { pf = pack8(p0[0], p0[1], p0[2], p0[3], p1[0], p1[1], p1[2], p1[3]); PIN(pf); }
      if constexpr (k == 12) { SB(); rescale(); }
      if constexpr (k == 21) {
        mx = max3(s0[0], s0[1], s0[2]);
        mx = max3(mx, s0[3], s1[0]); mx = max3(mx, s1[1], s1[2]); mx = __builtin_fmaxf(mx, s1[3]);
        mx = x16_max(xhalf_max(mx));
        SB(); decide(mx);
      }
      if constexpr (k == 22) { p0[0] = EXP2M(s0[0]); p0[1] = EXP2M(s0[1]); p0[2] = EXP2M(s0[2]); p0[3] = EXP2M(s0[3]); PIN(p0); }
      if constexpr (k == 23) { p1[0] = EXP2M(s1[0]); p1[1] = EXP2M(s1[1]); p1[2] = EXP2M(s1[2]); p1[3] = EXP2M(s1[3]); PIN(p1); }
      SB();
    });
    ks_rd = inc3(ks_rd); vs_rd = inc3(vs_rd);
  };
  auto newhead = [&]() {
#pragma unroll
    for (int s = 0; s < 6; ++s)
      qf[s] = pack8(qa[2 * s][0], qa[2 * s][1], qa[2 * s][2], qa[2 * s][3], qa[2 * s + 1][0], qa[2 * s + 1][1], qa[2 * s + 1][2], qa[2 * s + 1][3]);
#pragma unroll
    for (int T = 0; T < 12; ++T) qa[T] = f32x4{};
    f32x4 s0 = {}, s1 = {};
#pragma unroll
    for (int s = 0; s < 6; ++s) { bf16x8 a0 = KRD(2 * s), a1 = KRD(2 * s + 1); s0 = MFMA16(a0, qf[s], s0); s1 = MFMA16(a1, qf[s], s1); }
    float mx = max3(s0[0], s0[1], s0[2]);
    mx = max3(mx, s0[3], s1[0]); mx = max3(mx, s1[1], s1[2]); mx = __builtin_fmaxf(mx, s1[3]);
    m = x16_max(xhalf_max(mx)); mc = -m * QSCALE; l = 0.f; resc = false;
#pragma unroll
    for (int r = 0; r < 4; ++r) { p0[r] = EXP2M(s0[r]); p1[r] = EXP2M(s1[r]); }
    PIN(p0); PIN(p1);
    ks_rd = inc3(ks_rd);
  };

  using I0 = std::integral_constant<int, 0>; using I1 = std::integral_constant<int, 1>; using I2 = std::integral_constant<int, 2>;
  auto finish = [&](int e) {
#pragma unroll
    for (int k = 0; k < 8; ++k) o[4 + k] = MFMA16(fb[k], pf, o[4 + k]);
    rescale();
    l += (p0[0] + p0[1]) + (p0[2] + p0[3]) + (p1[0] + p1[1]) + (p1[2] + p1[3]);
    pf = pack8(p0[0], p0[1], p0[2], p0[3], p1[0], p1[1], p1[2], p1[3]);
#pragma unroll
    for (int T = 0; T < 12; ++T) { bf16x8 a = VRD(T); o[T] = MFMA16(a, pf, o[T]); }
    vs_rd = inc3(vs_rd);
    const float inv = 1.0f / x16_sum(xhalf_sum(l));
    char* op = c.og + (size_t)4 * 384 * 1024 + ((size_t)((c.h0 + e) * 6) * 64 + lane) * 16;
#pragma unroll
    for (int s = 0; s < 6; ++s) {
      bf16x8 f = pack8(o[2 * s][0] * inv, o[2 * s][1] * inv, o[2 * s][2] * inv, o[2 * s][3] * inv,
                       o[2 * s + 1][0] * inv, o[2 * s + 1][1] * inv, o[2 * s + 1][2] * inv, o[2 * s + 1][3] * inv);
      OSTORE(f, op + s * 1024);
      o[2 * s] = f32x4{}; o[2 * s + 1] = f32x4{};
    }
#pragma unroll
    for (int k = 0; k < 8; ++k) fb[k] = bf16x8{};
  };
  WAIT_BAR(63);
  for (int p = 0; p < 10; ++p) {
    const int pr = (p + 1) % 3;
    if (p == 4) touchw(1, 0);
    if (p == 7) touchw(1, 4);
    lds_char* const lsave = lds;
    lds = lsave + (pr == 2 ? -LDS_WX : 2 * pr * WXS);
    chunk(0); chunk(1);
    lds = lsave;
    WAIT_BAR(63);
  }
  WAIT_BAR(63);
  WAIT_BAR(63);
  newhead();
  WAIT_BAR(63);
  for (int e = 0; e < 3; ++e) {
    for (int mm = 0; mm < 5; ++mm) {
      auto tch = [&]() { const int g4 = mm + TOUCH_AHEAD; if (g4 < 5) touchw(e + 1, 4 * g4); else if (e < 2) touchw(e + 2, 4 * (g4 - 5)); };
      if (TPOS == 0) tch();
      stepq(I1{}, I0{}); WAIT_BAR(63);
      stepq(I1{}, I1{}); if (TPOS == 1) tch(); WAIT_BAR(63);
      stepq(I2{}, I2{}); WAIT_BAR(63);
    }
    finish(e);
    WAIT_BAR(63);
    WAIT_BAR(63);
    newhead();
    WAIT_BAR(63);
  }
  for (int i = 0; i < 15; ++i) { stepq(I0{}, I0{}); WAIT_BAR(63); }
  finish(3);
#undef KRD
#undef VRD
#undef WRD
#undef EXP2M
}

__global__ __launch_bounds__(512, 2) void attn_fused(const bf16* xA, const bf16* kA, const bf16* vA, const bf16* wqA,
                                                     const bf16* xB, const bf16* kB, const bf16* vB, const bf16* wqB, char* ows) {
  extern __shared__ __attribute__((aligned(16))) char smem[];
  lds_char* lds = (lds_char*)smem;
  const int g = blockIdx.x, xcd = g & 7, jj = g >> 3;
  const int br = xcd >> 2, bl = jj & 7, b = 8 * ((xcd >> 1) & 1) + bl, h0 = 4 * (4 * (xcd & 1) + (jj >> 3));
  const int wave = __builtin_amdgcn_readfirstlane(threadIdx.x >> 6), lane = threadIdx.x & 63;
  Ctx c;
  const size_t kvo = (size_t)(b * NH + h0) * SK * DH * 2;
  c.kg = (const char*)(br ? kB : kA) + kvo;
  c.vg = (const char*)(br ? vB : vA) + kvo;
  c.wq = (const char*)(br ? wqB : wqA) + h0 * DH * 2;
  c.xg = (const char*)(br ? xB : xA) + (size_t)b * SQ * DM * 2;
  c.og = ows + (size_t)(br * NB + b) * O_PER_B;
  c.h0 = h0; c.b = bl; c.xs = jj / NBX;
  if (threadIdx.x == 0) *(unsigned*)(ows + (size_t)2 * NB * O_PER_B + g * 128) = 0u;
  if (wave < 4) main_wave(c, lds, wave, lane);
  else if (wave == 4) {
    __builtin_amdgcn_s_setprio(2);
    q8_wave(c, lds, lane); }
  else proj_wave(c, lds, wave - 4, lane);
}

constexpr int P_WOB = 32 * 320;
constexpr int P_OB = 18 * 1024;
constexpr int P_CH = P_WOB + P_OB;
constexpr int P_NSLOT = 5, P_LDS = P_NSLOT * P_CH;
constexpr int P_NSTEP = 48, P_LA = 4;
#define P_PH (rot < 6 ? rot : 6)
constexpr size_t WS_O_BYTES = (size_t)2 * NB * O_PER_B;
constexpr size_t WS_FLAG = WS_O_BYTES;
constexpr size_t WS_XCH = WS_FLAG + 256 * 128;
constexpr int XCH_W = 20480 + 3072;
constexpr int XCH_BYTES = 4 * XCH_W;

DI void st_sc1(char* sb, unsigned vo, f32x4 v) { asm volatile("global_store_dwordx4 %0, %1, %2 sc1\n\ts_nop 1" :: "v"(vo), "v"(v), "s"(sb) : "memory"); }
DI f32x4 ld_sc1(const char* sb, unsigned vo) { f32x4 v; asm volatile("global_load_dwordx4 %0, %1, %2 sc1" : "=v"(v) : "v"(vo), "s"(sb) : "memory"); return v; }

__global__ __launch_bounds__(512, 2) void oproj(const char* ows, const bf16* woA, const bf16* woB, bf16* out, char* wsx) {
  extern __shared__ __attribute__((aligned(16))) char smem[];
  lds_char* lds = (lds_char*)smem;
  const unsigned lds0 = (unsigned)(uintptr_t)lds;
  const int g = blockIdx.x, xcd = g & 7, jj = g >> 3;
  const int br = xcd >> 2, kq = xcd & 3, bp = jj & 7, nq = jj >> 3;
  const int wave = __builtin_amdgcn_readfirstlane(threadIdx.x >> 6), lane = threadIdx.x & 63;
  const char* og = ows + (size_t)(br * NB + 2 * bp) * O_PER_B;
  const char* wo = (const char*)(br ? woB : woA) + (size_t)kq * 1536 * (DM * 2) + nq * 320;
  const int g0 = g & ~3;
  char* xbase = wsx + (WS_XCH - WS_FLAG);
  bf16* orow0 = out + (size_t)(2 * bp * SQ) * (2 * DM) + br * DM + nq * 160;
  const unsigned l16 = lane * 16;
#define P_BAR(N) do { SB(); asm volatile("s_waitcnt vmcnt(" #N ") lgkmcnt(0)\n\ts_barrier" ::: "memory"); SB(); } while (0)

#define P_BARM(N) do { SB(); asm volatile("s_waitcnt vmcnt(" #N ") lgkmcnt(0)\n\ts_barrier" ::: "memory"); SB(); } while (0)
  if (wave < 4) {
    const int w = wave, h2 = lane >> 5;
    const unsigned blk = (lane >> 4) & 1, q4 = (lane & 15) >> 2, p = lane & 3, gq = lane >> 4;
    const unsigned ab = 320u * (4u * h2 + q4) + 8u * (4u * (p & 1) + 2u * blk + (p >> 1));
    const unsigned bb = P_WOB + 4096u * w + l16;
    const int T0 = w < 2 ? 3 * w : 2 * w + 2;
    const unsigned ab8 = 320u * (4u * gq + q4) + 8u * p + 32u * T0;
    const unsigned bb8 = P_WOB + 16384u + l16;
    f32x16 acc[2][5];
#pragma unroll
    for (int t = 0; t < 5; ++t) { acc[0][t] = f32x16{}; acc[1][t] = f32x16{}; }
    f32x4 ac8[2][3] = {};
    bf16x8 a[5] = {}, bA[2] = {}, bB[2] = {};
    bf16x8 a8 = {}, b8[2] = {};
#define P_KS(br_, off, s, bm, X0, X1, X2, X3, X4) do { \
      const lds_char* pa_ = lds + (off) + ab + 5120 * (s); \
      acc[0][0] = MFMA32(a[0], bm[0], acc[0][0]); br_[0] = ldsr(lds + (off) + bb + 1024 * (s)); SB(); \
      acc[1][0] = MFMA32(a[0], bm[1], acc[1][0]); a[0] = tr2(pa_, pa_ + 2560); SB(); \
      acc[0][1] = MFMA32(a[1], bm[0], acc[0][1]); br_[1] = ldsr(lds + (off) + bb + 2048 + 1024 * (s)); X0; SB(); \
      acc[1][1] = MFMA32(a[1], bm[1], acc[1][1]); a[1] = tr2(pa_ + 64, pa_ + 2560 + 64); SB(); \
      acc[0][2] = MFMA32(a[2], bm[0], acc[0][2]); X1; SB(); \
      acc[1][2] = MFMA32(a[2], bm[1], acc[1][2]); a[2] = tr2(pa_ + 128, pa_ + 2560 + 128); SB(); \
      acc[0][3] = MFMA32(a[3], bm[0], acc[0][3]); X2; SB(); \
      acc[1][3] = MFMA32(a[3], bm[1], acc[1][3]); a[3] = tr2(pa_ + 192, pa_ + 2560 + 192); SB(); \
      acc[0][4] = MFMA32(a[4], bm[0], acc[0][4]); X3; SB(); \
      acc[1][4] = MFMA32(a[4], bm[1], acc[1][4]); a[4] = tr2(pa_ + 256, pa_ + 2560 + 256); X4; SB(); } while (0)
#define M8(t) (ac8[0][t] = MFMA16(a8, b8[0], ac8[0][t]), ac8[1][t] = MFMA16(a8, b8[1], ac8[1][t]))
#define R8(off, t) a8 = tr2(lds + (off) + ab8 + 32 * (t), lds + (off) + ab8 + 32 * (t) + 5120)
#define NOP (void)0
    P_BARM(63);
    int so = 0;
    for (int st = 0; st < P_NSTEP; ++st) {
      P_KS(bA, so, 0, bB, b8[0] = ldsr(lds + so + bb8), (b8[1] = ldsr(lds + so + bb8 + 1024), R8(so, 0)), NOP, (M8(0), R8(so, 1)), NOP);
      P_KS(bB, so, 1, bA, NOP, (M8(1), R8(so, 2)), NOP, M8(2), NOP);
      so = (so == (P_NSLOT - 1) * P_CH) ? 0 : so + P_CH;
      P_BARM(63);
    }
#pragma unroll
    for (int t = 0; t < 5; ++t) { acc[0][t] = MFMA32(a[t], bB[0], acc[0][t]); acc[1][t] = MFMA32(a[t], bB[1], acc[1][t]); }
#undef P_KS
#undef M8
#undef R8
#undef NOP
    unsigned ln;
    asm volatile("v_mbcnt_lo_u32_b32 %0, -1, 0\n\tv_mbcnt_hi_u32_b32 %0, -1, %0" : "=v"(ln));
#define l16 (ln * 16u)
#define lane ((int)ln)
#define h2 ((int)(ln >> 5))
#define gq (ln >> 4)
    {
      char* xs = xbase + (size_t)g0 * XCH_BYTES + kq * XCH_W + w * 5120;
      lds_char* own = lds + w * 10240 + l16;
#pragma unroll
      for (int pt = 0; pt < 4; ++pt) {
        if (pt == kq) {
#pragma unroll
          for (int i = 0; i < 10; ++i) {
            const int gi = 10 * pt + i; const f32x16& a = acc[gi / 20][(gi % 20) >> 2];
            f32x4 v = {a[4 * (gi & 3)], a[4 * (gi & 3) + 1], a[4 * (gi & 3) + 2], a[4 * (gi & 3) + 3]};
            *(__attribute__((address_space(3))) f32x4*)(own + i * 1024) = v;
          }
        } else {
#pragma unroll
          for (int i = 0; i < 5; ++i) {
            const int gi = 10 * pt + 2 * i; const f32x16& a = acc[gi / 20][(gi % 20) >> 2]; const int r0 = 4 * (gi & 3);
            bf16x8 f = pack8(a[r0], a[r0 + 1], a[r0 + 2], a[r0 + 3], a[r0 + 4], a[r0 + 5], a[r0 + 6], a[r0 + 7]);
            st_sc1(xs + (size_t)pt * XCH_BYTES + i * 1024, l16, __builtin_bit_cast(f32x4, f));
          }
        }
      }
      if (w != kq) {
        char* x8 = xbase + (size_t)(g0 + w) * XCH_BYTES + kq * XCH_W + 20480;
#pragma unroll
        for (int t = 0; t < 3; ++t) {
          bf16x8 f = pack8(ac8[0][t][0], ac8[0][t][1], ac8[0][t][2], ac8[0][t][3], ac8[1][t][0], ac8[1][t][1], ac8[1][t][2], ac8[1][t][3]);
          st_sc1(x8 + t * 1024, l16, __builtin_bit_cast(f32x4, f));
        }
      }
    }
    P_BARM(0);
    if (w == 0) {
      if (lane == 0) __hip_atomic_store((unsigned*)(wsx + g * 128), 1u, __ATOMIC_RELAXED, __HIP_MEMORY_SCOPE_AGENT);
      unsigned* pf = (unsigned*)(wsx + (g0 + (lane & 3)) * 128);
      for (int spin = 0; spin < (1 << 20); ++spin) {
        if (!__any(__hip_atomic_load(pf, __ATOMIC_RELAXED, __HIP_MEMORY_SCOPE_AGENT) == 0u)) break;
        __builtin_amdgcn_s_sleep(1);
      }
    }
    P_BARM(0);
    {
      const char* xr = xbase + (size_t)g * XCH_BYTES;
      f32x4 v[15], v8[9];
#pragma unroll
      for (int i = 0; i < 15; ++i) v[i] = ld_sc1(xr + ((kq + 1 + i / 5) & 3) * XCH_W + w * 5120 + (i % 5) * 1024, l16);
      if (w == kq) {
#pragma unroll
        for (int i = 0; i < 9; ++i) v8[i] = ld_sc1(xr + ((kq + 1 + i / 3) & 3) * XCH_W + 20480 + (i % 3) * 1024, l16);
      } else {
#pragma unroll
        for (int i = 0; i < 9; ++i) v8[i] = f32x4{};
      }
      asm volatile("s_waitcnt vmcnt(0)" : "+v"(v[0]), "+v"(v[1]), "+v"(v[2]), "+v"(v[3]), "+v"(v[4]), "+v"(v[5]), "+v"(v[6]), "+v"(v[7]), "+v"(v[8]), "+v"(v[9]),
                   "+v"(v[10]), "+v"(v[11]), "+v"(v[12]), "+v"(v[13]), "+v"(v[14]) :: "memory");
      asm volatile("" : "+v"(v8[0]), "+v"(v8[1]), "+v"(v8[2]), "+v"(v8[3]), "+v"(v8[4]), "+v"(v8[5]), "+v"(v8[6]), "+v"(v8[7]), "+v"(v8[8]) :: "memory");
      auto lo = [](float x) { return __uint_as_float(__float_as_uint(x) << 16); };
      auto hi = [](float x) { return __uint_as_float(__float_as_uint(x) & 0xffff0000u); };
      const lds_char* own = lds + w * 10240 + l16;
      bf16* orow = orow0 + (size_t)((w >> 1) * SQ + 64 * (w & 1) + 32 * (kq >> 1) + (lane & 31)) * (2 * DM) + 16 * h2;
#pragma unroll
      for (int pr = 0; pr < 5; ++pr) {
        f32x4 a = *(const __attribute__((address_space(3))) f32x4*)(own + (2 * pr) * 1024);
        f32x4 c2 = *(const __attribute__((address_space(3))) f32x4*)(own + (2 * pr + 1) * 1024);
#pragma unroll
        for (int sidx = 0; sidx < 3; ++sidx) {
          const f32x4 x = v[5 * sidx + pr];
          a[0] += lo(x[0]); a[1] += hi(x[0]); a[2] += lo(x[1]); a[3] += hi(x[1]);
          c2[0] += lo(x[2]); c2[1] += hi(x[2]); c2[2] += lo(x[3]); c2[3] += hi(x[3]);
        }
        const int gi = 10 * (kq & 1) + 2 * pr;
        bf16x8 f = pack8(a[0], a[1], a[2], a[3], c2[0], c2[1], c2[2], c2[3]);
        *(bf16x8*)(orow + 32 * (gi >> 2) + 4 * (gi & 3)) = f;
      }
      if (w == kq) {
#pragma unroll
        for (int t = 0; t < 3; ++t) {
          if (t < 2 || w < 2) {
            f32x4 a = ac8[0][t], c2 = ac8[1][t];
#pragma unroll
            for (int sidx = 0; sidx < 3; ++sidx) {
              const f32x4 x = v8[3 * sidx + t];
              a[0] += lo(x[0]); a[1] += hi(x[0]); a[2] += lo(x[1]); a[3] += hi(x[1]);
              c2[0] += lo(x[2]); c2[1] += hi(x[2]); c2[2] += lo(x[3]); c2[3] += hi(x[3]);
            }
            bf16* orow8 = orow0 + (size_t)(128 + (lane & 15)) * (2 * DM) + 16 * (T0 + t) + 4 * gq;
            uint2 f0 = {cvtpk(a[0], a[1]), cvtpk(a[2], a[3])}, f1 = {cvtpk(c2[0], c2[1]), cvtpk(c2[2], c2[3])};
            *(uint2*)orow8 = f0; *(uint2*)(orow8 + (size_t)SQ * (2 * DM)) = f1;
          }
        }
      }
    }
#undef l16
#undef lane
#undef h2
#undef gq
  } else {
    __builtin_amdgcn_s_setprio(P_PRIO);
    const int j = wave - 4;
    unsigned wofs[3];
#pragma unroll
    for (int ii = 0; ii < 3; ++ii) { unsigned pp = 64u * (j + 4 * ii) + lane; unsigned row = pp / 20; wofs[ii] = row * (DM * 2) + (pp - 20 * row) * 16; }
    const char* op = og + (size_t)(j >> 1) * O_PER_B + (size_t)(2 * (j & 1) * 384 + kq * 96) * 1024;
    const char* o8 = og + (size_t)(j & 1) * O_PER_B + (size_t)4 * 384 * 1024 + (size_t)(kq * 48) * 1024;
    const int rot = (2 * nq + bp) & 7, ph = P_PH;
    int tab;
    {
      const int sl = lane;
      if (sl < 2) tab = (rot + sl) & 7;
      else {
        const int u = sl - 2, blk8 = u >> 3, r = u & 7;
        if (blk8 < 5 && r >= ph && r < ph + 2) tab = 8 * (blk8 + 1) + ((rot + r - ph) & 7);
        else tab = 8 * blk8 + ((rot + 2 + ((blk8 < 5 && r >= ph) ? r - 2 : r)) & 7);
      }
    }
    int it = 0;
    auto issue = [&](unsigned d) {
      const int ks = __builtin_amdgcn_readlane(tab, it);
      const char* wp = wo + (size_t)ks * (32 * DM * 2);
      dma16<0, 0>(wp, wofs[0], d + 1024u * j); dma16<0, 4096>(wp, wofs[1], d + 1024u * j);
      if (j < 2) dma16<0, 8192>(wp, wofs[2], d + 1024u * j);
      const char* oq = op + ks * 2048;
      dma16<0, P_WOB>(oq, l16, d + 4096u * j); dma16<0, P_WOB + 1024>(oq + 1024, l16, d + 4096u * j);
      dma16<0, P_WOB + 2048>(oq + 384 * 1024, l16, d + 4096u * j); dma16<0, P_WOB + 3072>(oq + 385 * 1024, l16, d + 4096u * j);
      if (j >= 2) dma16<0, P_WOB + 16384>(o8 + ks * 1024, l16, d + 1024u * (j & 1));
      ++it;
    };
    issue(lds0); issue(lds0 + P_CH);
    P_BAR(7);
    issue(lds0 + 2 * P_CH); issue(lds0 + 3 * P_CH);
    P_BAR(14);
    issue(lds0 + 4 * P_CH); issue(lds0);
    P_BAR(21);
    int sw = P_CH;
    for (int st = 2; st < P_NSTEP; ++st) {
      const int rel = (st + P_LA - 2 - ph) & 7;
      const bool inl = (st + P_LA - 2 - ph) >= 0 && (st + P_LA - 2 - ph) < 40 && P_SKIP;
      int n = 1;
      if (inl && rel == 2 && st + P_LA - 2 - ph >= 8) n = 0;
      if (inl && rel == 4 && st + P_LA - 2 - ph >= 8) n = 2;
      for (int q = 0; q < n; ++q)
        if (it < P_NSTEP) { issue(lds0 + sw); sw = (sw == (P_NSLOT - 1) * P_CH) ? 0 : sw + P_CH; }
      const int ahead = it - (st + 2);
      if (ahead >= 3) { P_BAR(21); } else if (ahead == 2) { P_BAR(14); } else if (ahead == 1) { P_BAR(7); } else { P_BAR(0); }
    }
    P_BAR(0);
    P_BAR(0);
  }
#undef P_BAR
}

extern "C" void kernel_launch(void* const* d_in, const int* in_sizes, int n_in, void* d_out, int out_size, void* d_ws, size_t ws_size,
                              hipStream_t stream) {
  (void)hipFuncSetAttribute((const void*)attn_fused, hipFuncAttributeMaxDynamicSharedMemorySize, LDS_TOTAL);
  (void)hipFuncSetAttribute((const void*)oproj, hipFuncAttributeMaxDynamicSharedMemorySize, P_LDS);
  attn_fused<<<256, 512, LDS_TOTAL, stream>>>((const bf16*)d_in[0], (const bf16*)d_in[1], (const bf16*)d_in[2], (const bf16*)d_in[3],
                                              (const bf16*)d_in[5], (const bf16*)d_in[6], (const bf16*)d_in[7], (const bf16*)d_in[8], (char*)d_ws);
  oproj<<<256, 512, P_LDS, stream>>>((const char*)d_ws, (const bf16*)d_in[4], (const bf16*)d_in[9], (bf16*)d_out, (char*)d_ws + WS_FLAG);
}
```
